```python
import torch

def _softmax(x, axis=-1):
    return torch.softmax(x, dim=axis)
def _gelu(x):
    return torch.nn.functional.gelu(x, approximate="tanh")
def _silu(x):
    return torch.nn.functional.silu(x)
def _sigmoid(x):
    return torch.sigmoid(x)
def _layernorm(x, axis=-1, eps=1e-5):
    m = x.mean(dim=axis, keepdim=True)
    v = x.var(dim=axis, keepdim=True, unbiased=False)
    return (x - m) / torch.sqrt(v + eps)
def _scatter_add(tgt, idx, src, axis=0):
    return tgt.clone().index_add_(axis, idx, src)
def _segment_sum(src, seg, n):
    out = torch.zeros((n,) + src.shape[1:], dtype=src.dtype)
    return out.index_add_(0, seg, src)
def _pad(x, pw):
    flat = tuple(v for pair in reversed(pw) for v in pair)
    return torch.nn.functional.pad(x, flat)

kernel_name = 'concat_heads__softmax_full_mha__sliding_block__prompt'

def setup_inputs():

    _w = (lambda a: (lambda t: t.to(torch.bfloat16) if t.is_floating_point() else t)(torch.as_tensor(a)))
    def _A_setup():
        B, S, D = 1, 2048, 512
        return {
            "x": _w((lambda *s: torch.randn(*s, dtype=torch.bfloat16) * 0.2)(B, S, D)),
            "wqkv": _w((lambda *s: torch.randn(*s, dtype=torch.bfloat16) * 0.4)(D, 3 * D)),
            "wo": _w((lambda *s: torch.randn(*s, dtype=torch.bfloat16) * 0.1)(D, D)),
        }
    _A_in = _A_setup()

    def _B_setup():
        B, S, D = 1, 2048, 512
        return {
            "x": _w((lambda *s: torch.randn(*s, dtype=torch.bfloat16) * 0.4)(B, S, D)),
            "wqkv": _w((lambda *s: torch.randn(*s, dtype=torch.bfloat16) * 0.4)(D, 3 * D)),
            "wo": _w((lambda *s: torch.randn(*s, dtype=torch.bfloat16) * 0.05)(D, D)),
        }
    _B_in = _B_setup()

    return {
        "A_x": _A_in["x"],
        "A_wqkv": _A_in["wqkv"],
        "A_wo": _A_in["wo"],
        "B_x": _B_in["x"],
        "B_wqkv": _B_in["wqkv"],
        "B_wo": _B_in["wo"],
    }

def reference(A_x, A_wqkv, A_wo, B_x, B_wqkv, B_wo):
    def _A_ref(x, wqkv, wo):
        B, S, D = x.shape
        H = 32; Dh = D // H
        qkv = (lambda x, *d: x.permute(*d))((x @ wqkv).reshape(B, S, 3, H, Dh), 2, 0, 3, 1, 4)
        q, k, v = qkv[0], qkv[1], qkv[2]
        scale = Dh ** -0.5
        scores = torch.einsum("bhqd,bhkd->bhqk", q, k) * scale
        p = _softmax(scores, axis=-1)
        o = torch.einsum("bhqk,bhkd->bhqd", p, v)
        return (lambda x, *d: x.permute(*d))(o, 0, 2, 1, 3).reshape(B, S, D) @ wo

    def _B_ref(x, wqkv, wo):
        B, S, D = x.shape
        H = 32; Dh = D // H; W = 256
        qkv = (lambda x, *d: x.permute(*d))((x @ wqkv).reshape(B, S, 3, H, Dh), 2, 0, 3, 1, 4)
        q, k, v = qkv[0], qkv[1], qkv[2]
        scores = torch.einsum("bhqd,bhkd->bhqk", q, k) * (Dh ** -0.5)
        idx = torch.arange(S)
        mask = (idx[:, None] - idx[None, :] >= 0) & (idx[:, None] - idx[None, :] < W)
        scores = torch.where(mask[None, None], scores, -1e9)
        p = _softmax(scores, axis=-1)
        o = torch.einsum("bhqk,bhkd->bhqd", p, v)
        return (lambda x, *d: x.permute(*d))(o, 0, 2, 1, 3).reshape(B, S, D) @ wo

    a = _A_ref(A_x, A_wqkv, A_wo)
    b = _B_ref(B_x, B_wqkv, B_wo)
    return torch.concatenate([a, b], axis=-1)
```

```cpp
#include <hip/hip_runtime.h>
#include <hip/hip_bf16.h>
#include <stdint.h>

typedef short bf16x8 __attribute__((ext_vector_type(8)));
typedef short s16x4 __attribute__((ext_vector_type(4)));
typedef float f32x16 __attribute__((ext_vector_type(16)));
typedef float f32x2 __attribute__((ext_vector_type(2)));
typedef __bf16 b16x2 __attribute__((ext_vector_type(2)));
typedef unsigned u32x4 __attribute__((ext_vector_type(4)));
typedef unsigned u32x2 __attribute__((ext_vector_type(2)));
typedef const __attribute__((address_space(3))) char* lds_cptr;
#define LDSP(T) __attribute__((address_space(3))) T*
#define DI __device__ __forceinline__
#define MFMA(a, b, c) __builtin_amdgcn_mfma_f32_32x32x16_bf16(a, b, c, 0, 0, 0)

constexpr int SEQ = 2048, DM = 512, NH = 32;
constexpr size_t WS_Q = 0x000000, WS_K = 0x400000, WS_V = 0x800000, WS_O = 0xD00000;
constexpr size_t QK_BR = 0x200000, V_BR = 0x240000;
constexpr int VTILE = 1088, VHEAD = 64 * VTILE;
constexpr float QSCALE = 0.25f * 1.4426950408889634f;

DI unsigned cvtpk(float lo, float hi) { f32x2 v = {lo, hi}; b16x2 b = __builtin_convertvector(v, b16x2); return __builtin_bit_cast(unsigned, b); }
DI void glds16(const void* sbase, unsigned voff, unsigned lds_dst) {
    unsigned keep;
    asm volatile("s_mov_b32 %0, m0\n\ts_mov_b32 m0, %3\n\ts_nop 0\n\tglobal_load_lds_dwordx4 %1, %2\n\ts_mov_b32 m0, %0"
                 : "=&s"(keep) : "v"(voff), "s"(sbase), "s"(lds_dst) : "memory");
}
DI bf16x8 lds_b128(lds_cptr p) { return *(const LDSP(bf16x8))p; }
DI s16x4 lds_tr(lds_cptr p) { return __builtin_amdgcn_ds_read_tr16_b64_v4i16((LDSP(s16x4))p); }
DI bf16x8 cat8(s16x4 a, s16x4 b) { return (bf16x8){a[0], a[1], a[2], a[3], b[0], b[1], b[2], b[3]}; }
DI u32x4 pack8(const f32x16& a, int o, float sc) {
    return (u32x4){cvtpk(a[o] * sc, a[o + 1] * sc), cvtpk(a[o + 2] * sc, a[o + 3] * sc), cvtpk(a[o + 4] * sc, a[o + 5] * sc), cvtpk(a[o + 6] * sc, a[o + 7] * sc)};
}
#define WAIT_VM(n) asm volatile("s_waitcnt vmcnt(" #n ")" ::: "memory")
#define BARRIER() asm volatile("s_waitcnt lgkmcnt(0)\n\ts_barrier" ::: "memory")

constexpr int QKV_STAGE = 28672;
__global__ __launch_bounds__(256, 2) void qkv_kernel(const uint16_t* xA, const uint16_t* wA, const uint16_t* xB, const uint16_t* wB, char* ws) {
    extern __shared__ __attribute__((aligned(16))) char smem[];
    const int bid = blockIdx.x, br = bid >> 8, rb = (bid >> 4) & 15, hp = bid & 15;
    const uint16_t* x = br ? xB : xA; const uint16_t* w = br ? wB : wA;
    const int lane = threadIdx.x & 63, wv = __builtin_amdgcn_readfirstlane(threadIdx.x >> 6), hi = lane >> 5, l31 = lane & 31;
    const unsigned lds0 = (unsigned)(uintptr_t)(LDSP(char))smem;
    unsigned voffx[4], voffw[3];
    { const int gsw = (4 * (wv & 1) + (lane >> 4)) & 7;
#pragma unroll
      for (int m = 0; m < 4; ++m) voffx[m] = (8 * (wv + 4 * m) + (lane >> 3)) * 1024 + ((lane & 7) ^ gsw) * 16;
#pragma unroll
      for (int j = 0; j < 3; ++j) { const int off = j * 1024 + lane * 16, row = 16 * wv + off / 192, c = (off % 192) >> 4; voffw[j] = row * 3072 + (c >> 2) * 1024 + hp * 64 + (c & 3) * 16; } }
    const char* xb = (const char*)x + (size_t)rb * 128 * 1024; const char* wb = (const char*)w;
    auto stage = [&](int kc, int st) {
        const unsigned d = lds0 + st * QKV_STAGE;
#pragma unroll
        for (int m = 0; m < 4; ++m) glds16(xb + kc * 128, voffx[m], d + (wv + 4 * m) * 1024);
#pragma unroll
        for (int j = 0; j < 3; ++j) glds16(wb + (size_t)kc * 64 * 3072, voffw[j], d + 16384 + (3 * wv + j) * 1024);
    };
    const int xrow = 32 * wv + l31; const unsigned xaddr0 = xrow * 128 + ((hi ^ ((l31 >> 1) & 7)) << 4);
    const unsigned wbase = 16384 + (8 * hi + ((lane >> 2) & 3)) * 192 + ((lane >> 4) & 1) * 32 + (lane & 3) * 8;
    f32x16 acc[3] = {};
    stage(0, 0);
    for (int kc = 0; kc < 8; ++kc) {
        WAIT_VM(0); BARRIER();
        if (kc + 1 < 8) stage(kc + 1, (kc + 1) & 1);
        lds_cptr sb = (lds_cptr)smem + (kc & 1) * QKV_STAGE;
#pragma unroll
        for (int ks = 0; ks < 4; ++ks) {
            const bf16x8 xf = lds_b128(sb + (xaddr0 ^ (ks << 5)));
            bf16x8 wf[3];
#pragma unroll
            for (int ft = 0; ft < 3; ++ft) wf[ft] = cat8(lds_tr(sb + wbase + ks * 16 * 192 + ft * 64), lds_tr(sb + wbase + (ks * 16 + 4) * 192 + ft * 64));
            acc[0] = MFMA(wf[0], xf, acc[0]); acc[1] = MFMA(wf[1], xf, acc[1]); acc[2] = MFMA(xf, wf[2], acc[2]);
        }
    }
    const int tok = rb * 128 + xrow;
    char* qp = ws + WS_Q + br * QK_BR + ((size_t)(2 * hp) * SEQ + tok) * 32 + hi * 16; char* kp = qp + (WS_K - WS_Q);
    *(u32x4*)qp = pack8(acc[0], 0, QSCALE); *(u32x4*)(qp + SEQ * 32) = pack8(acc[0], 8, QSCALE);
    *(u32x4*)kp = pack8(acc[1], 0, 1.f); *(u32x4*)(kp + SEQ * 32) = pack8(acc[1], 8, 1.f);
    char* vp = ws + WS_V + br * V_BR + (size_t)(2 * hp + (l31 >> 4)) * VHEAD + (rb * 4 + wv) * VTILE + hi * 272 + (l31 & 15) * 16;
    *(u32x4*)vp = pack8(acc[2], 0, 1.f); *(u32x4*)(vp + 544) = pack8(acc[2], 8, 1.f);
    if ((l31 & 15) == 0) { const u32x4 ones = {0x3F803F80u, 0x3F803F80u, 0x3F803F80u, 0x3F803F80u}; *(u32x4*)(vp + 256) = ones; *(u32x4*)(vp + 544 + 256) = ones; }
}

DI float rowmax16(const f32x16& s) {
    float a = fmaxf(fmaxf(s[0], s[1]), s[2]), b = fmaxf(fmaxf(s[3], s[4]), s[5]);
    a = fmaxf(fmaxf(a, s[6]), s[7]); b = fmaxf(fmaxf(b, s[8]), s[9]); a = fmaxf(fmaxf(a, s[10]), s[11]); b = fmaxf(fmaxf(b, s[12]), s[13]); a = fmaxf(fmaxf(a, s[14]), s[15]);
    const float m = fmaxf(a, b); auto rr = __builtin_amdgcn_permlane32_swap(__float_as_uint(m), __float_as_uint(m), false, false);
    return fmaxf(__uint_as_float(rr[0]), __uint_as_float(rr[1]));
}
DI f32x16 splat16(float v) { f32x16 r;
#pragma unroll
    for (int i = 0; i < 16; ++i) r[i] = v; return r; }
DI void exp_pack(f32x16& s, bf16x8& p0, bf16x8& p1) {
#pragma unroll
    for (int i = 0; i < 16; ++i) s[i] = __builtin_amdgcn_exp2f(s[i]);
    p0 = __builtin_bit_cast(bf16x8, pack8(s, 0, 1.f)); p1 = __builtin_bit_cast(bf16x8, pack8(s, 8, 1.f));
}
constexpr int AT_K = 0, AT_V = 65536, AT_X = AT_V + VHEAD, AT_LDS = AT_X + 8 * 2304;

__global__ __launch_bounds__(512, 2) void attn_kernel(char* ws) {
    extern __shared__ __attribute__((aligned(16))) char smem[];
    const int bid = blockIdx.x, h = (bid & 7) * 4 + ((bid >> 3) & 3), qb = bid >> 5;
    const int lane = threadIdx.x & 63, wv = __builtin_amdgcn_readfirstlane(threadIdx.x >> 6), hi = lane >> 5, l31 = lane & 31;
    const unsigned lds0 = (unsigned)(uintptr_t)(LDSP(char))smem;
    const int q0 = qb * 256;
    const unsigned kbase = l31 * 32 + hi * 16, vbase = hi * 272 + (l31 < 16 ? l31 : 16) * 16;
    {
        const char* Kg = ws + WS_K + QK_BR + (size_t)h * SEQ * 32; const char* Vg = ws + WS_V + V_BR + (size_t)h * VHEAD;
        const int t0 = q0 / 32 - 8;
        for (int i = wv; i < 16; i += 8) if (t0 + i >= 0) glds16(Kg + (size_t)(t0 + i) * 1024, lane * 16, lds0 + AT_K + i * 1024);
        { const int vs = t0 < 0 ? 8 : 0;
          const char* vsrc = Vg + (size_t)(t0 + vs) * VTILE; const int nb = (16 - vs) * VTILE;
          for (int i = wv; i * 1024 < nb; i += 8) { if (i * 1024 + lane * 16 < nb) glds16(vsrc + i * 1024, lane * 16, lds0 + AT_V + vs * VTILE + i * 1024); } }
        const int qw = q0 + 32 * wv;
        const bf16x8 qf = *(const bf16x8*)(ws + WS_Q + QK_BR + ((size_t)h * SEQ + qw + l31) * 32 + hi * 16);
        WAIT_VM(0); BARRIER();
        lds_cptr kp = (lds_cptr)smem + AT_K + kbase + wv * 1024, vp = (lds_cptr)smem + AT_V + vbase + wv * VTILE;
        const int tmin = (t0 + wv < 0) ? -(t0 + wv) : 0;
        f32x16 mF, mL;
#pragma unroll
        for (int r = 0; r < 16; ++r) { const int j = (r & 3) + 8 * (r >> 2) + 4 * hi; mF[r] = j > l31 ? 0.f : -INFINITY; mL[r] = j <= l31 ? 0.f : -INFINITY; }
        float m = rowmax16(MFMA(lds_b128(kp + 8 * 1024), qf, mL));
        for (int t = (tmin < 1 ? 1 : tmin); t < 8; ++t) m = fmaxf(m, rowmax16(MFMA(lds_b128(kp + t * 1024), qf, splat16(0.f))));
        if (tmin == 0) m = fmaxf(m, rowmax16(MFMA(lds_b128(kp), qf, mF)));
        f32x16 o = {}; const f32x16 cm = splat16(-m);
        for (int t = tmin; t <= 8; ++t) {
            f32x16 c = cm;
            if (t == 0) {
#pragma unroll
                for (int r = 0; r < 16; ++r) c[r] += mF[r]; }
            if (t == 8) {
#pragma unroll
                for (int r = 0; r < 16; ++r) c[r] += mL[r]; }
            f32x16 s = MFMA(lds_b128(kp + t * 1024), qf, c); bf16x8 p0, p1; exp_pack(s, p0, p1);
            o = MFMA(lds_b128(vp + t * VTILE), p0, o); o = MFMA(lds_b128(vp + t * VTILE + 544), p1, o);
        }
        const float rl = 1.f / o[8];
        *(u32x4*)(ws + WS_O + QK_BR + ((size_t)h * SEQ + qw + l31) * 32 + hi * 16) = pack8(o, 0, rl);
        BARRIER();
    }
    {
        const char* Kg = ws + WS_K + (size_t)h * SEQ * 32; const char* Vg = ws + WS_V + (size_t)h * VHEAD;
        for (int i = wv; i < 64; i += 8) glds16(Kg + i * 1024, lane * 16, lds0 + AT_K + i * 1024);
        for (int i = wv; i < 68; i += 8) glds16(Vg + i * 1024, lane * 16, lds0 + AT_V + i * 1024);
        const int qg = wv & 3, kh = wv >> 2, qw = q0 + 64 * qg;
        const char* qp = ws + WS_Q + ((size_t)h * SEQ + qw + l31) * 32 + hi * 16;
        const bf16x8 qa = *(const bf16x8*)qp, qbf = *(const bf16x8*)(qp + 1024);
        WAIT_VM(0); BARRIER();
        lds_cptr kp = (lds_cptr)smem + AT_K + kbase, vp = (lds_cptr)smem + AT_V + vbase;
        const bf16x8 k0 = lds_b128(kp);
        const f32x16 ca = splat16(-rowmax16(MFMA(k0, qa, splat16(0.f)))), cb = splat16(-rowmax16(MFMA(k0, qbf, splat16(0.f))));
        kp += kh * 32 * 1024; vp += kh * 32 * VTILE;
        f32x16 oa = {}, ob = {};
#pragma unroll 2
        for (int t = 0; t < 32; ++t) {
            const bf16x8 kf = lds_b128(kp + t * 1024), v0 = lds_b128(vp + t * VTILE), v1 = lds_b128(vp + t * VTILE + 544);
            f32x16 sa = MFMA(kf, qa, ca), sb = MFMA(kf, qbf, cb); bf16x8 p0, p1;
            exp_pack(sa, p0, p1); oa = MFMA(v0, p0, oa); oa = MFMA(v1, p1, oa);
            exp_pack(sb, p0, p1); ob = MFMA(v0, p0, ob); ob = MFMA(v1, p1, ob);
        }
        LDSP(float) xs = (LDSP(float))(smem + AT_X) + wv * 576; LDSP(float) xr = (LDSP(float))(smem + AT_X) + (wv ^ 4) * 576;
        const f32x16& snd = kh ? oa : ob;
#pragma unroll
        for (int r = 0; r < 9; ++r) xs[r * 64 + lane] = kh ? oa[r] : ob[r];
        (void)snd;
        BARRIER();
        f32x16 o = kh ? ob : oa;
#pragma unroll
        for (int r = 0; r < 9; ++r) o[r] += xr[r * 64 + lane];
        const float rl = 1.f / o[8];
        *(u32x4*)(ws + WS_O + ((size_t)h * SEQ + qw + 32 * kh + l31) * 32 + hi * 16) = pack8(o, 0, rl);
    }
}

__global__ __launch_bounds__(256, 2) void oproj_kernel(const uint16_t* woA, const uint16_t* woB, const char* ws, uint16_t* out) {
    extern __shared__ __attribute__((aligned(16))) char smem[];
    const int bid = blockIdx.x, br = bid >> 7, rb = (bid >> 3) & 15, cb = bid & 7;
    const int lane = threadIdx.x & 63, wv = __builtin_amdgcn_readfirstlane(threadIdx.x >> 6), hi = lane >> 5, l31 = lane & 31;
    const unsigned lds0 = (unsigned)(uintptr_t)(LDSP(char))smem;
    const char* wo = (const char*)(br ? woB : woA) + cb * 128;
    const unsigned voff = (lane >> 3) * 1024 + (((lane & 7) ^ (((lane >> 4) & 1) << 2)) << 4);
    for (int n = wv; n < 64; n += 4) glds16(wo + n * 8192, voff, lds0 + n * 1024);
    const int tok = rb * 128 + 32 * wv + l31;
    const char* op = ws + WS_O + br * QK_BR + (size_t)tok * 32 + hi * 16;
    const int q = (lane >> 2) & 3;
    unsigned wb[2];
#pragma unroll
    for (int ct = 0; ct < 2; ++ct) wb[ct] = (4 * hi + q) * 128 + ((ct ^ (q >> 1)) * 64) + ((lane >> 4) & 1) * 32 + (lane & 3) * 8;
    WAIT_VM(0); BARRIER();
    f32x16 acc[2] = {};
#pragma unroll 8
    for (int hh = 0; hh < 32; ++hh) {
        const bf16x8 of = *(const bf16x8*)(op + (size_t)hh * SEQ * 32);
#pragma unroll
        for (int ct = 0; ct < 2; ++ct) { lds_cptr p = (lds_cptr)smem + wb[ct] + hh * 2048; acc[ct] = MFMA(cat8(lds_tr(p), lds_tr(p + 1024)), of, acc[ct]); }
    }
    uint16_t* orow = out + (size_t)tok * 1024 + br * 512 + cb * 64 + 4 * hi;
#pragma unroll
    for (int ct = 0; ct < 2; ++ct)
#pragma unroll
        for (int g = 0; g < 4; ++g) *(u32x2*)(orow + ct * 32 + 8 * g) = (u32x2){cvtpk(acc[ct][4 * g], acc[ct][4 * g + 1]), cvtpk(acc[ct][4 * g + 2], acc[ct][4 * g + 3])};
}

extern "C" void kernel_launch(void* const* d_in, const int* in_sizes, int n_in, void* d_out, int out_size, void* d_ws, size_t ws_size, hipStream_t stream) {
    const uint16_t *xA = (const uint16_t*)d_in[0], *wA = (const uint16_t*)d_in[1], *woA = (const uint16_t*)d_in[2], *xB = (const uint16_t*)d_in[3], *wB = (const uint16_t*)d_in[4], *woB = (const uint16_t*)d_in[5];
    char* ws = (char*)d_ws;
    (void)hipFuncSetAttribute((const void*)attn_kernel, hipFuncAttributeMaxDynamicSharedMemorySize, AT_LDS);
    (void)hipFuncSetAttribute((const void*)qkv_kernel, hipFuncAttributeMaxDynamicSharedMemorySize, 2 * QKV_STAGE);
    (void)hipFuncSetAttribute((const void*)oproj_kernel, hipFuncAttributeMaxDynamicSharedMemorySize, 65536);
    qkv_kernel<<<512, 256, 2 * QKV_STAGE, stream>>>(xA, wA, xB, wB, ws);
    attn_kernel<<<256, 512, AT_LDS, stream>>>(ws);
    oproj_kernel<<<256, 256, 65536, stream>>>(woA, woB, ws, (uint16_t*)d_out);
}
```

```cpp
#include <hip/hip_runtime.h>
#include <stdint.h>
#define QKV_FIRST_ASM \
    "s_waitcnt lgkmcnt(0)\n\t" \
    "s_barrier\n\t" \
    "v_add_u32 v185, %[soff], v180\n\t" \
    "v_add_u32 v181, %[soff], v176\n\t" \
    "v_add_u32 v182, %[soff], v177\n\t" \
    "v_add_u32 v183, %[soff], v178\n\t" \
    "v_add_u32 v184, %[soff], v179\n\t" \
    "ds_read_b128 v[48:51], v181\n\t" \
    "ds_read_b64_tr_b16 v[64:65], v185 offset:0\n\t" \
    "ds_read_b64_tr_b16 v[66:67], v185 offset:1536\n\t" \
    "ds_read_b64_tr_b16 v[68:69], v185 offset:128\n\t" \
    "ds_read_b64_tr_b16 v[70:71], v185 offset:1664\n\t" \
    "ds_read_b64_tr_b16 v[72:73], v185 offset:256\n\t" \
    "ds_read_b64_tr_b16 v[74:75], v185 offset:1792\n\t" \
    "ds_read_b128 v[52:55], v182\n\t" \
    "ds_read_b64_tr_b16 v[76:77], v185 offset:6144\n\t" \
    "ds_read_b64_tr_b16 v[78:79], v185 offset:7680\n\t" \
    "ds_read_b64_tr_b16 v[80:81], v185 offset:6272\n\t" \
    "ds_read_b64_tr_b16 v[82:83], v185 offset:7808\n\t" \
    "ds_read_b64_tr_b16 v[84:85], v185 offset:6400\n\t" \
    "ds_read_b64_tr_b16 v[86:87], v185 offset:7936\n\t" \
    "ds_read_b128 v[56:59], v183\n\t" \
    "ds_read_b64_tr_b16 v[88:89], v185 offset:12288\n\t" \
    "ds_read_b64_tr_b16 v[90:91], v185 offset:13824\n\t" \
    "ds_read_b64_tr_b16 v[92:93], v185 offset:12416\n\t" \
    "ds_read_b64_tr_b16 v[94:95], v185 offset:13952\n\t" \
    "ds_read_b64_tr_b16 v[96:97], v185 offset:12544\n\t" \
    "ds_read_b64_tr_b16 v[98:99], v185 offset:14080\n\t" \
    "ds_read_b128 v[60:63], v184\n\t" \
    "ds_read_b64_tr_b16 v[100:101], v185 offset:18432\n\t" \
    "ds_read_b64_tr_b16 v[102:103], v185 offset:19968\n\t" \
    "ds_read_b64_tr_b16 v[104:105], v185 offset:18560\n\t" \
    "ds_read_b64_tr_b16 v[106:107], v185 offset:20096\n\t" \
    "ds_read_b64_tr_b16 v[108:109], v185 offset:18688\n\t" \
    "ds_read_b64_tr_b16 v[110:111], v185 offset:20224\n\t" \
    "s_waitcnt lgkmcnt(0)\n\t"

#define QKV_PHASE_ASM \
    "1:\n\t" \
    "s_waitcnt lgkmcnt(0)\n\t" \
    "s_barrier\n\t" \
    "v_mfma_f32_32x32x16_bf16 v[0:15], v[64:67], v[48:51], v[0:15]\n\t" \
    "s_add_u32 %[soff], %[soff], 0x8000\n\t" \
    "s_and_b32 %[soff], %[soff], 0x18000\n\t" \
    "v_add_u32 v185, %[soff], v180\n\t" \
    "v_add_u32 v181, %[soff], v176\n\t" \
    "v_add_u32 v182, %[soff], v177\n\t" \
    "v_add_u32 v183, %[soff], v178\n\t" \
    "v_add_u32 v184, %[soff], v179\n\t" \
    "ds_read_b128 v[112:115], v181\n\t" \
    "ds_read_b64_tr_b16 v[128:129], v185 offset:0\n\t" \
    "ds_read_b64_tr_b16 v[130:131], v185 offset:1536\n\t" \
    "v_mfma_f32_32x32x16_bf16 v[16:31], v[68:71], v[48:51], v[16:31]\n\t" \
    "ds_read_b64_tr_b16 v[132:133], v185 offset:128\n\t" \
    "ds_read_b64_tr_b16 v[134:135], v185 offset:1664\n\t" \
    "ds_read_b64_tr_b16 v[136:137], v185 offset:256\n\t" \
    "v_mfma_f32_32x32x16_bf16 v[32:47], v[48:51], v[72:75], v[32:47]\n\t" \
    "ds_read_b64_tr_b16 v[138:139], v185 offset:1792\n\t" \
    "ds_read_b128 v[116:119], v182\n\t" \
    "ds_read_b64_tr_b16 v[140:141], v185 offset:6144\n\t" \
    "v_mfma_f32_32x32x16_bf16 v[0:15], v[76:79], v[52:55], v[0:15]\n\t" \
    "ds_read_b64_tr_b16 v[142:143], v185 offset:7680\n\t" \
    "ds_read_b64_tr_b16 v[144:145], v185 offset:6272\n\t" \
    "ds_read_b64_tr_b16 v[146:147], v185 offset:7808\n\t" \
    "v_mfma_f32_32x32x16_bf16 v[16:31], v[80:83], v[52:55], v[16:31]\n\t" \
    "ds_read_b64_tr_b16 v[148:149], v185 offset:6400\n\t" \
    "ds_read_b64_tr_b16 v[150:151], v185 offset:7936\n\t" \
    "ds_read_b128 v[120:123], v183\n\t" \
    "v_mfma_f32_32x32x16_bf16 v[32:47], v[52:55], v[84:87], v[32:47]\n\t" \
    "ds_read_b64_tr_b16 v[152:153], v185 offset:12288\n\t" \
    "ds_read_b64_tr_b16 v[154:155], v185 offset:13824\n\t" \
    "ds_read_b64_tr_b16 v[156:157], v185 offset:12416\n\t" \
    "v_mfma_f32_32x32x16_bf16 v[0:15], v[88:91], v[56:59], v[0:15]\n\t" \
    "ds_read_b64_tr_b16 v[158:159], v185 offset:13952\n\t" \
    "ds_read_b64_tr_b16 v[160:161], v185 offset:12544\n\t" \
    "ds_read_b64_tr_b16 v[162:163], v185 offset:14080\n\t" \
    "v_mfma_f32_32x32x16_bf16 v[16:31], v[92:95], v[56:59], v[16:31]\n\t" \
    "ds_read_b128 v[124:127], v184\n\t" \
    "ds_read_b64_tr_b16 v[164:165], v185 offset:18432\n\t" \
    "v_mfma_f32_32x32x16_bf16 v[32:47], v[56:59], v[96:99], v[32:47]\n\t" \
    "ds_read_b64_tr_b16 v[166:167], v185 offset:19968\n\t" \
    "ds_read_b64_tr_b16 v[168:169], v185 offset:18560\n\t" \
    "v_mfma_f32_32x32x16_bf16 v[0:15], v[100:103], v[60:63], v[0:15]\n\t" \
    "ds_read_b64_tr_b16 v[170:171], v185 offset:20096\n\t" \
    "ds_read_b64_tr_b16 v[172:173], v185 offset:18688\n\t" \
    "v_mfma_f32_32x32x16_bf16 v[16:31], v[104:107], v[60:63], v[16:31]\n\t" \
    "ds_read_b64_tr_b16 v[174:175], v185 offset:20224\n\t" \
    "v_mfma_f32_32x32x16_bf16 v[32:47], v[60:63], v[108:111], v[32:47]\n\t" \
    "s_waitcnt lgkmcnt(0)\n\t" \
    "s_barrier\n\t" \
    "v_mfma_f32_32x32x16_bf16 v[0:15], v[128:131], v[112:115], v[0:15]\n\t" \
    "s_add_u32 %[soff], %[soff], 0x8000\n\t" \
    "s_and_b32 %[soff], %[soff], 0x18000\n\t" \
    "v_add_u32 v185, %[soff], v180\n\t" \
    "v_add_u32 v181, %[soff], v176\n\t" \
    "v_add_u32 v182, %[soff], v177\n\t" \
    "v_add_u32 v183, %[soff], v178\n\t" \
    "v_add_u32 v184, %[soff], v179\n\t" \
    "ds_read_b128 v[48:51], v181\n\t" \
    "ds_read_b64_tr_b16 v[64:65], v185 offset:0\n\t" \
    "ds_read_b64_tr_b16 v[66:67], v185 offset:1536\n\t" \
    "v_mfma_f32_32x32x16_bf16 v[16:31], v[132:135], v[112:115], v[16:31]\n\t" \
    "ds_read_b64_tr_b16 v[68:69], v185 offset:128\n\t" \
    "ds_read_b64_tr_b16 v[70:71], v185 offset:1664\n\t" \
    "ds_read_b64_tr_b16 v[72:73], v185 offset:256\n\t" \
    "v_mfma_f32_32x32x16_bf16 v[32:47], v[112:115], v[136:139], v[32:47]\n\t" \
    "ds_read_b64_tr_b16 v[74:75], v185 offset:1792\n\t" \
    "ds_read_b128 v[52:55], v182\n\t" \
    "ds_read_b64_tr_b16 v[76:77], v185 offset:6144\n\t" \
    "v_mfma_f32_32x32x16_bf16 v[0:15], v[140:143], v[116:119], v[0:15]\n\t" \
    "ds_read_b64_tr_b16 v[78:79], v185 offset:7680\n\t" \
    "ds_read_b64_tr_b16 v[80:81], v185 offset:6272\n\t" \
    "ds_read_b64_tr_b16 v[82:83], v185 offset:7808\n\t" \
    "v_mfma_f32_32x32x16_bf16 v[16:31], v[144:147], v[116:119], v[16:31]\n\t" \
    "ds_read_b64_tr_b16 v[84:85], v185 offset:6400\n\t" \
    "ds_read_b64_tr_b16 v[86:87], v185 offset:7936\n\t" \
    "ds_read_b128 v[56:59], v183\n\t" \
    "v_mfma_f32_32x32x16_bf16 v[32:47], v[116:119], v[148:151], v[32:47]\n\t" \
    "ds_read_b64_tr_b16 v[88:89], v185 offset:12288\n\t" \
    "ds_read_b64_tr_b16 v[90:91], v185 offset:13824\n\t" \
    "ds_read_b64_tr_b16 v[92:93], v185 offset:12416\n\t" \
    "v_mfma_f32_32x32x16_bf16 v[0:15], v[152:155], v[120:123], v[0:15]\n\t" \
    "ds_read_b64_tr_b16 v[94:95], v185 offset:13952\n\t" \
    "ds_read_b64_tr_b16 v[96:97], v185 offset:12544\n\t" \
    "ds_read_b64_tr_b16 v[98:99], v185 offset:14080\n\t" \
    "v_mfma_f32_32x32x16_bf16 v[16:31], v[156:159], v[120:123], v[16:31]\n\t" \
    "ds_read_b128 v[60:63], v184\n\t" \
    "ds_read_b64_tr_b16 v[100:101], v185 offset:18432\n\t" \
    "v_mfma_f32_32x32x16_bf16 v[32:47], v[120:123], v[160:163], v[32:47]\n\t" \
    "ds_read_b64_tr_b16 v[102:103], v185 offset:19968\n\t" \
    "ds_read_b64_tr_b16 v[104:105], v185 offset:18560\n\t" \
    "v_mfma_f32_32x32x16_bf16 v[0:15], v[164:167], v[124:127], v[0:15]\n\t" \
    "ds_read_b64_tr_b16 v[106:107], v185 offset:20096\n\t" \
    "ds_read_b64_tr_b16 v[108:109], v185 offset:18688\n\t" \
    "v_mfma_f32_32x32x16_bf16 v[16:31], v[168:171], v[124:127], v[16:31]\n\t" \
    "ds_read_b64_tr_b16 v[110:111], v185 offset:20224\n\t" \
    "v_mfma_f32_32x32x16_bf16 v[32:47], v[124:127], v[172:175], v[32:47]\n\t" \
    "s_sub_u32 %[cnt], %[cnt], 1\n\t" \
    "s_cmp_lg_u32 %[cnt], 0\n\t" \
    "s_cbranch_scc1 1b\n\t" \
    "s_waitcnt lgkmcnt(0)\n\t" \
    "s_nop 15\n\t"

#define QKV_CLOB_B "v112","v113","v114","v115","v116","v117","v118","v119","v120","v121","v122","v123","v124","v125","v126","v127","v128","v129","v130","v131","v132","v133","v134","v135","v136","v137","v138","v139","v140","v141","v142","v143","v144","v145","v146","v147","v148","v149","v150","v151","v152","v153","v154","v155","v156","v157","v158","v159","v160","v161","v162","v163","v164","v165","v166","v167","v168","v169","v170","v171","v172","v173","v174","v175","v181","v182","v183","v184","v185"

#define ATTN_A_ASM \
    "v_mov_b32 v124, v138\n\t" \
    "v_mov_b32 v125, v138\n\t" \
    "v_mov_b32 v126, v138\n\t" \
    "v_mov_b32 v127, v138\n\t" \
    "v_mov_b32 v128, v138\n\t" \
    "v_mov_b32 v129, v138\n\t" \
    "v_mov_b32 v130, v138\n\t" \
    "v_mov_b32 v131, v138\n\t" \
    "v_mov_b32 v132, v138\n\t" \
    "v_mov_b32 v133, v138\n\t" \
    "v_mov_b32 v134, v138\n\t" \
    "v_mov_b32 v135, v138\n\t" \
    "v_mov_b32 v108, 0\n\t" \
    "v_mov_b32 v109, 0\n\t" \
    "v_mov_b32 v110, 0\n\t" \
    "v_mov_b32 v111, 0\n\t" \
    "ds_read_b128 v[120:123], v136\n\t" \
    "s_mov_b64 exec, %[vmask]\n\t" \
    "ds_read_b128 v[124:127], v137\n\t" \
    "ds_read_b128 v[128:131], v137 offset:512\n\t" \
    "s_mov_b64 exec, -1\n\t" \
    "s_waitcnt lgkmcnt(2)\n\t" \
    "v_mfma_f32_32x32x16_bf16 v[64:79], v[120:123], v[112:115], v[32:47]\n\t" \
    "s_nop 15\n\t" \
    "1:\n\t" \
    "v_exp_f32 v64, v64\n\t" \
    "v_exp_f32 v65, v65\n\t" \
    "v_exp_f32 v66, v66\n\t" \
    "v_cvt_pk_bf16_f32 v96, v64, v65\n\t" \
    "v_exp_f32 v67, v67\n\t" \
    "v_exp_f32 v68, v68\n\t" \
    "v_cvt_pk_bf16_f32 v97, v66, v67\n\t" \
    "v_exp_f32 v69, v69\n\t" \
    "v_mfma_f32_32x32x16_bf16 v[80:95], v[120:123], v[116:119], v[48:63]\n\t" \
    "ds_read_b128 v[120:123], v136 offset:2048\n\t" \
    "v_exp_f32 v70, v70\n\t" \
    "v_cvt_pk_bf16_f32 v98, v68, v69\n\t" \
    "v_exp_f32 v71, v71\n\t" \
    "v_exp_f32 v72, v72\n\t" \
    "v_cvt_pk_bf16_f32 v99, v70, v71\n\t" \
    "v_exp_f32 v73, v73\n\t" \
    "v_exp_f32 v74, v74\n\t" \
    "v_cvt_pk_bf16_f32 v100, v72, v73\n\t" \
    "s_waitcnt lgkmcnt(2)\n\t" \
    "v_mfma_f32_32x32x16_bf16 v[0:15], v[124:127], v[96:99], v[0:15]\n\t" \
    "v_exp_f32 v75, v75\n\t" \
    "v_exp_f32 v76, v76\n\t" \
    "v_cvt_pk_bf16_f32 v101, v74, v75\n\t" \
    "v_exp_f32 v77, v77\n\t" \
    "v_exp_f32 v78, v78\n\t" \
    "v_exp_f32 v79, v79\n\t" \
    "v_cvt_pk_bf16_f32 v102, v76, v77\n\t" \
    "v_cvt_pk_bf16_f32 v103, v78, v79\n\t" \
    "v_mfma_f32_32x32x16_bf16 v[16:31], v[132:135], v[108:111], v[16:31]\n\t" \
    "v_exp_f32 v80, v80\n\t" \
    "v_exp_f32 v81, v81\n\t" \
    "v_exp_f32 v82, v82\n\t" \
    "v_cvt_pk_bf16_f32 v104, v80, v81\n\t" \
    "v_exp_f32 v83, v83\n\t" \
    "v_exp_f32 v84, v84\n\t" \
    "v_cvt_pk_bf16_f32 v105, v82, v83\n\t" \
    "v_exp_f32 v85, v85\n\t" \
    "s_waitcnt lgkmcnt(0)\n\t" \
    "v_mfma_f32_32x32x16_bf16 v[64:79], v[120:123], v[112:115], v[32:47]\n\t" \
    "v_exp_f32 v86, v86\n\t" \
    "v_cvt_pk_bf16_f32 v106, v84, v85\n\t" \
    "v_exp_f32 v87, v87\n\t" \
    "v_exp_f32 v88, v88\n\t" \
    "v_cvt_pk_bf16_f32 v107, v86, v87\n\t" \
    "v_exp_f32 v89, v89\n\t" \
    "v_exp_f32 v90, v90\n\t" \
    "v_cvt_pk_bf16_f32 v108, v88, v89\n\t" \
    "v_mfma_f32_32x32x16_bf16 v[16:31], v[124:127], v[104:107], v[16:31]\n\t" \
    "s_mov_b64 exec, %[vmask]\n\t" \
    "ds_read_b128 v[124:127], v137 offset:2048\n\t" \
    "ds_read_b128 v[132:135], v137 offset:2560\n\t" \
    "s_mov_b64 exec, -1\n\t" \
    "v_exp_f32 v91, v91\n\t" \
    "v_exp_f32 v92, v92\n\t" \
    "v_cvt_pk_bf16_f32 v109, v90, v91\n\t" \
    "v_exp_f32 v93, v93\n\t" \
    "v_exp_f32 v94, v94\n\t" \
    "v_exp_f32 v95, v95\n\t" \
    "v_cvt_pk_bf16_f32 v110, v92, v93\n\t" \
    "v_cvt_pk_bf16_f32 v111, v94, v95\n\t" \
    "v_mfma_f32_32x32x16_bf16 v[0:15], v[128:131], v[100:103], v[0:15]\n\t" \
    "v_exp_f32 v64, v64\n\t" \
    "v_exp_f32 v65, v65\n\t" \
    "v_exp_f32 v66, v66\n\t" \
    "v_cvt_pk_bf16_f32 v96, v64, v65\n\t" \
    "v_exp_f32 v67, v67\n\t" \
    "v_exp_f32 v68, v68\n\t" \
    "v_cvt_pk_bf16_f32 v97, v66, v67\n\t" \
    "v_exp_f32 v69, v69\n\t" \
    "v_mfma_f32_32x32x16_bf16 v[80:95], v[120:123], v[116:119], v[48:63]\n\t" \
    "ds_read_b128 v[120:123], v136 offset:4096\n\t" \
    "v_exp_f32 v70, v70\n\t" \
    "v_cvt_pk_bf16_f32 v98, v68, v69\n\t" \
    "v_exp_f32 v71, v71\n\t" \
    "v_exp_f32 v72, v72\n\t" \
    "v_cvt_pk_bf16_f32 v99, v70, v71\n\t" \
    "v_exp_f32 v73, v73\n\t" \
    "v_exp_f32 v74, v74\n\t" \
    "v_cvt_pk_bf16_f32 v100, v72, v73\n\t" \
    "s_waitcnt lgkmcnt(2)\n\t" \
    "v_mfma_f32_32x32x16_bf16 v[0:15], v[124:127], v[96:99], v[0:15]\n\t" \
    "v_exp_f32 v75, v75\n\t" \
    "v_exp_f32 v76, v76\n\t" \
    "v_cvt_pk_bf16_f32 v101, v74, v75\n\t" \
    "v_exp_f32 v77, v77\n\t" \
    "v_exp_f32 v78, v78\n\t" \
    "v_exp_f32 v79, v79\n\t" \
    "v_cvt_pk_bf16_f32 v102, v76, v77\n\t" \
    "v_cvt_pk_bf16_f32 v103, v78, v79\n\t" \
    "v_mfma_f32_32x32x16_bf16 v[16:31], v[128:131], v[108:111], v[16:31]\n\t" \
    "v_exp_f32 v80, v80\n\t" \
    "v_exp_f32 v81, v81\n\t" \
    "v_exp_f32 v82, v82\n\t" \
    "v_cvt_pk_bf16_f32 v104, v80, v81\n\t" \
    "v_exp_f32 v83, v83\n\t" \
    "v_exp_f32 v84, v84\n\t" \
    "v_cvt_pk_bf16_f32 v105, v82, v83\n\t" \
    "v_exp_f32 v85, v85\n\t" \
    "s_waitcnt lgkmcnt(0)\n\t" \
    "v_mfma_f32_32x32x16_bf16 v[64:79], v[120:123], v[112:115], v[32:47]\n\t" \
    "v_exp_f32 v86, v86\n\t" \
    "v_cvt_pk_bf16_f32 v106, v84, v85\n\t" \
    "v_exp_f32 v87, v87\n\t" \
    "v_exp_f32 v88, v88\n\t" \
    "v_cvt_pk_bf16_f32 v107, v86, v87\n\t" \
    "v_exp_f32 v89, v89\n\t" \
    "v_exp_f32 v90, v90\n\t" \
    "v_cvt_pk_bf16_f32 v108, v88, v89\n\t" \
    "v_mfma_f32_32x32x16_bf16 v[16:31], v[124:127], v[104:107], v[16:31]\n\t" \
    "s_mov_b64 exec, %[vmask]\n\t" \
    "ds_read_b128 v[124:127], v137 offset:4096\n\t" \
    "ds_read_b128 v[128:131], v137 offset:4608\n\t" \
    "s_mov_b64 exec, -1\n\t" \
    "v_exp_f32 v91, v91\n\t" \
    "v_exp_f32 v92, v92\n\t" \
    "v_cvt_pk_bf16_f32 v109, v90, v91\n\t" \
    "v_exp_f32 v93, v93\n\t" \
    "v_exp_f32 v94, v94\n\t" \
    "v_exp_f32 v95, v95\n\t" \
    "v_cvt_pk_bf16_f32 v110, v92, v93\n\t" \
    "v_cvt_pk_bf16_f32 v111, v94, v95\n\t" \
    "v_mfma_f32_32x32x16_bf16 v[0:15], v[132:135], v[100:103], v[0:15]\n\t" \
    "v_exp_f32 v64, v64\n\t" \
    "v_exp_f32 v65, v65\n\t" \
    "v_exp_f32 v66, v66\n\t" \
    "v_cvt_pk_bf16_f32 v96, v64, v65\n\t" \
    "v_exp_f32 v67, v67\n\t" \
    "v_exp_f32 v68, v68\n\t" \
    "v_cvt_pk_bf16_f32 v97, v66, v67\n\t" \
    "v_exp_f32 v69, v69\n\t" \
    "v_mfma_f32_32x32x16_bf16 v[80:95], v[120:123], v[116:119], v[48:63]\n\t" \
    "ds_read_b128 v[120:123], v136 offset:6144\n\t" \
    "v_exp_f32 v70, v70\n\t" \
    "v_cvt_pk_bf16_f32 v98, v68, v69\n\t" \
    "v_exp_f32 v71, v71\n\t" \
    "v_exp_f32 v72, v72\n\t" \
    "v_cvt_pk_bf16_f32 v99, v70, v71\n\t" \
    "v_exp_f32 v73, v73\n\t" \
    "v_exp_f32 v74, v74\n\t" \
    "v_cvt_pk_bf16_f32 v100, v72, v73\n\t" \
    "s_waitcnt lgkmcnt(2)\n\t" \
    "v_mfma_f32_32x32x16_bf16 v[0:15], v[124:127], v[96:99], v[0:15]\n\t" \
    "v_exp_f32 v75, v75\n\t" \
    "v_exp_f32 v76, v76\n\t" \
    "v_cvt_pk_bf16_f32 v101, v74, v75\n\t" \
    "v_exp_f32 v77, v77\n\t" \
    "v_exp_f32 v78, v78\n\t" \
    "v_exp_f32 v79, v79\n\t" \
    "v_cvt_pk_bf16_f32 v102, v76, v77\n\t" \
    "v_cvt_pk_bf16_f32 v103, v78, v79\n\t" \
    "v_mfma_f32_32x32x16_bf16 v[16:31], v[132:135], v[108:111], v[16:31]\n\t" \
    "v_exp_f32 v80, v80\n\t" \
    "v_exp_f32 v81, v81\n\t" \
    "v_exp_f32 v82, v82\n\t" \
    "v_cvt_pk_bf16_f32 v104, v80, v81\n\t" \
    "v_exp_f32 v83, v83\n\t" \
    "v_exp_f32 v84, v84\n\t" \
    "v_cvt_pk_bf16_f32 v105, v82, v83\n\t" \
    "v_exp_f32 v85, v85\n\t" \
    "s_waitcnt lgkmcnt(0)\n\t" \
    "v_mfma_f32_32x32x16_bf16 v[64:79], v[120:123], v[112:115], v[32:47]\n\t" \
    "v_exp_f32 v86, v86\n\t" \
    "v_cvt_pk_bf16_f32 v106, v84, v85\n\t" \
    "v_exp_f32 v87, v87\n\t" \
    "v_exp_f32 v88, v88\n\t" \
    "v_cvt_pk_bf16_f32 v107, v86, v87\n\t" \
    "v_exp_f32 v89, v89\n\t" \
    "v_exp_f32 v90, v90\n\t" \
    "v_cvt_pk_bf16_f32 v108, v88, v89\n\t" \
    "v_mfma_f32_32x32x16_bf16 v[16:31], v[124:127], v[104:107], v[16:31]\n\t" \
    "s_mov_b64 exec, %[vmask]\n\t" \
    "ds_read_b128 v[124:127], v137 offset:6144\n\t" \
    "ds_read_b128 v[132:135], v137 offset:6656\n\t" \
    "s_mov_b64 exec, -1\n\t" \
    "v_exp_f32 v91, v91\n\t" \
    "v_exp_f32 v92, v92\n\t" \
    "v_cvt_pk_bf16_f32 v109, v90, v91\n\t" \
    "v_exp_f32 v93, v93\n\t" \
    "v_exp_f32 v94, v94\n\t" \
    "v_exp_f32 v95, v95\n\t" \
    "v_cvt_pk_bf16_f32 v110, v92, v93\n\t" \
    "v_cvt_pk_bf16_f32 v111, v94, v95\n\t" \
    "v_mfma_f32_32x32x16_bf16 v[0:15], v[128:131], v[100:103], v[0:15]\n\t" \
    "v_exp_f32 v64, v64\n\t" \
    "v_exp_f32 v65, v65\n\t" \
    "v_exp_f32 v66, v66\n\t" \
    "v_cvt_pk_bf16_f32 v96, v64, v65\n\t" \
    "v_exp_f32 v67, v67\n\t" \
    "v_exp_f32 v68, v68\n\t" \
    "v_cvt_pk_bf16_f32 v97, v66, v67\n\t" \
    "v_exp_f32 v69, v69\n\t" \
    "v_mfma_f32_32x32x16_bf16 v[80:95], v[120:123], v[116:119], v[48:63]\n\t" \
    "ds_read_b128 v[120:123], v136 offset:8192\n\t" \
    "v_exp_f32 v70, v70\n\t" \
    "v_cvt_pk_bf16_f32 v98, v68, v69\n\t" \
    "v_exp_f32 v71, v71\n\t" \
    "v_exp_f32 v72, v72\n\t" \
    "v_cvt_pk_bf16_f32 v99, v70, v71\n\t" \
    "v_exp_f32 v73, v73\n\t" \
    "v_exp_f32 v74, v74\n\t" \
    "v_cvt_pk_bf16_f32 v100, v72, v73\n\t" \
    "s_waitcnt lgkmcnt(2)\n\t" \
    "v_mfma_f32_32x32x16_bf16 v[0:15], v[124:127], v[96:99], v[0:15]\n\t" \
    "v_exp_f32 v75, v75\n\t" \
    "v_exp_f32 v76, v76\n\t" \
    "v_cvt_pk_bf16_f32 v101, v74, v75\n\t" \
    "v_exp_f32 v77, v77\n\t" \
    "v_exp_f32 v78, v78\n\t" \
    "v_exp_f32 v79, v79\n\t" \
    "v_cvt_pk_bf16_f32 v102, v76, v77\n\t" \
    "v_cvt_pk_bf16_f32 v103, v78, v79\n\t" \
    "v_mfma_f32_32x32x16_bf16 v[16:31], v[128:131], v[108:111], v[16:31]\n\t" \
    "v_exp_f32 v80, v80\n\t" \
    "v_exp_f32 v81, v81\n\t" \
    "v_exp_f32 v82, v82\n\t" \
    "v_cvt_pk_bf16_f32 v104, v80, v81\n\t" \
    "v_exp_f32 v83, v83\n\t" \
    "v_exp_f32 v84, v84\n\t" \
    "v_cvt_pk_bf16_f32 v105, v82, v83\n\t" \
    "v_exp_f32 v85, v85\n\t" \
    "s_waitcnt lgkmcnt(0)\n\t" \
    "v_mfma_f32_32x32x16_bf16 v[64:79], v[120:123], v[112:115], v[32:47]\n\t" \
    "v_exp_f32 v86, v86\n\t" \
    "v_cvt_pk_bf16_f32 v106, v84, v85\n\t" \
    "v_exp_f32 v87, v87\n\t" \
    "v_exp_f32 v88, v88\n\t" \
    "v_cvt_pk_bf16_f32 v107, v86, v87\n\t" \
    "v_exp_f32 v89, v89\n\t" \
    "v_exp_f32 v90, v90\n\t" \
    "v_cvt_pk_bf16_f32 v108, v88, v89\n\t" \
    "v_mfma_f32_32x32x16_bf16 v[16:31], v[124:127], v[104:107], v[16:31]\n\t" \
    "s_mov_b64 exec, %[vmask]\n\t" \
    "ds_read_b128 v[124:127], v137 offset:8192\n\t" \
    "ds_read_b128 v[128:131], v137 offset:8704\n\t" \
    "s_mov_b64 exec, -1\n\t" \
    "v_exp_f32 v91, v91\n\t" \
    "v_exp_f32 v92, v92\n\t" \
    "v_cvt_pk_bf16_f32 v109, v90, v91\n\t" \
    "v_exp_f32 v93, v93\n\t" \
    "v_exp_f32 v94, v94\n\t" \
    "v_exp_f32 v95, v95\n\t" \
    "v_cvt_pk_bf16_f32 v110, v92, v93\n\t" \
    "v_cvt_pk_bf16_f32 v111, v94, v95\n\t" \
    "v_mfma_f32_32x32x16_bf16 v[0:15], v[132:135], v[100:103], v[0:15]\n\t" \
    "v_add_u32 v136, 0x2000, v136\n\t" \
    "v_add_u32 v137, 0x2000, v137\n\t" \
    "s_cmp_lg_u32 %[cnt], 16\n\t" \
    "s_cbranch_scc1 2f\n\t" \
    "s_waitcnt vmcnt(0)\n\t" \
    "s_mov_b64 exec, 1\n\t" \
    "global_store_dwordx2 v139, v[140:141], %[fbase] sc1\n\t" \
    "s_mov_b64 exec, -1\n\t" \
    "2:\n\t" \
    "s_sub_u32 %[cnt], %[cnt], 1\n\t" \
    "s_cmp_lg_u32 %[cnt], 0\n\t" \
    "s_cbranch_scc1 1b\n\t" \
    "s_nop 1\n\t" \
    "v_mfma_f32_32x32x16_bf16 v[16:31], v[132:135], v[108:111], v[16:31]\n\t" \
    "s_waitcnt lgkmcnt(0)\n\t" \
    "s_nop 15\n\t"

#define ATTN_A_CLOB "v64","v65","v66","v67","v68","v69","v70","v71","v72","v73","v74","v75","v76","v77","v78","v79","v80","v81","v82","v83","v84","v85","v86","v87","v88","v89","v90","v91","v92","v93","v94","v95","v96","v97","v98","v99","v100","v101","v102","v103","v104","v105","v106","v107","v108","v109","v110","v111","v120","v121","v122","v123","v124","v125","v126","v127","v128","v129","v130","v131","v132","v133","v134","v135"

typedef short bf16x8 __attribute__((ext_vector_type(8)));
typedef short s16x4 __attribute__((ext_vector_type(4)));
typedef float f32x16 __attribute__((ext_vector_type(16)));
typedef float f32x2 __attribute__((ext_vector_type(2)));
typedef __bf16 b16x2 __attribute__((ext_vector_type(2)));
typedef unsigned u32x4 __attribute__((ext_vector_type(4)));
typedef unsigned u32x2 __attribute__((ext_vector_type(2)));
typedef unsigned long long u64;
typedef const __attribute__((address_space(3))) char* lds_cptr;
#define LDSP(T) __attribute__((address_space(3))) T*
#define GASP(T) __attribute__((address_space(1))) T*
#define DI __device__ __forceinline__
#define MFMA(a, b, c) __builtin_amdgcn_mfma_f32_32x32x16_bf16(a, b, c, 0, 0, 0)
#define WAIT_VM(n) asm volatile("s_waitcnt vmcnt(" #n ")" ::: "memory")
#define BARRIER() asm volatile("s_waitcnt lgkmcnt(0)\n\ts_barrier" ::: "memory")

constexpr int SEQ = 2048;
constexpr unsigned WS_FQ = 0x1000, WS_FO = 0x8000, WS_Q = 0x100000, WS_KV = 0x500000, WS_O = 0xD00000, WS_BYTES = 0x1100000;
constexpr unsigned Q_BR = 0x200000, KV_BR = 0x400000, Q_HEAD = 0x10000, KV_HEAD = 0x20000;
constexpr float QSCALE = 0.25f * 1.4426950408889634f;
constexpr int STAGE = 32768, LDS_SPARE = 131072, LDS_BYTES = 139264;
constexpr unsigned SPIN_CAP = 1u << 20;
#define RSTAMP(slot) do {} while (0)

DI unsigned cvtpk(float lo, float hi) { f32x2 v = {lo, hi}; b16x2 b = __builtin_convertvector(v, b16x2); return __builtin_bit_cast(unsigned, b); }
template <int SC1> DI void glds16(const void* sbase, unsigned voff, unsigned lds_dst) {
    unsigned keep;
    if (SC1) asm volatile("s_mov_b32 %0, m0\n\ts_mov_b32 m0, %3\n\ts_nop 0\n\tglobal_load_lds_dwordx4 %1, %2 sc1\n\ts_mov_b32 m0, %0" : "=&s"(keep) : "v"(voff), "s"(sbase), "s"(lds_dst) : "memory");
    else asm volatile("s_mov_b32 %0, m0\n\ts_mov_b32 m0, %3\n\ts_nop 0\n\tglobal_load_lds_dwordx4 %1, %2\n\ts_mov_b32 m0, %0" : "=&s"(keep) : "v"(voff), "s"(sbase), "s"(lds_dst) : "memory");
}
DI void touch(const void* sbase, unsigned voff, unsigned lds_dst) {
    unsigned keep;
    asm volatile("s_mov_b32 %0, m0\n\ts_mov_b32 m0, %3\n\ts_nop 0\n\tglobal_load_lds_dword %1, %2\n\ts_mov_b32 m0, %0" : "=&s"(keep) : "v"(voff), "s"(sbase), "s"(lds_dst) : "memory");
}
DI bf16x8 lds_b128(lds_cptr p) { return *(const LDSP(bf16x8))p; }
DI s16x4 lds_tr(lds_cptr p) { return __builtin_amdgcn_ds_read_tr16_b64_v4i16((LDSP(s16x4))p); }
DI bf16x8 cat8(s16x4 a, s16x4 b) { return (bf16x8){a[0], a[1], a[2], a[3], b[0], b[1], b[2], b[3]}; }
DI u32x4 pack8(const f32x16& a, int o, float sc) {
    return (u32x4){cvtpk(a[o] * sc, a[o + 1] * sc), cvtpk(a[o + 2] * sc, a[o + 3] * sc), cvtpk(a[o + 4] * sc, a[o + 5] * sc), cvtpk(a[o + 6] * sc, a[o + 7] * sc)};
}
typedef __amdgpu_buffer_rsrc_t rsrc_t;
DI bf16x8 ld_sc1(rsrc_t r, unsigned off) { return __builtin_bit_cast(bf16x8, __builtin_amdgcn_raw_buffer_load_b128(r, off, 0, 16)); }
DI void st_sc1(rsrc_t r, unsigned off, u32x4 v) { __builtin_amdgcn_raw_buffer_store_b128(v, r, off, 0, 16); }
DI u64 flag_ld(const char* ws, unsigned off) { return __hip_atomic_load((GASP(u64))(ws + off), __ATOMIC_RELAXED, __HIP_MEMORY_SCOPE_AGENT); }
DI void flag_st(char* ws, unsigned off, u64 v) { __hip_atomic_store((GASP(u64))(ws + off), v, __ATOMIC_RELAXED, __HIP_MEMORY_SCOPE_AGENT); }
DI void wait_flags(const char* ws, unsigned off, bool active, u64 tag) {
#pragma unroll 1
    for (unsigned it = 0; it < SPIN_CAP; ++it) {
        const u64 v = active ? flag_ld(ws, off) : tag;
        if (__all(v == tag)) break;
        __builtin_amdgcn_s_sleep(1);
    }
    asm volatile("" ::: "memory");
}
DI float rowmax16(const f32x16& s) {
    float a = fmaxf(fmaxf(s[0], s[1]), s[2]), b = fmaxf(fmaxf(s[3], s[4]), s[5]);
    a = fmaxf(fmaxf(a, s[6]), s[7]); b = fmaxf(fmaxf(b, s[8]), s[9]); a = fmaxf(fmaxf(a, s[10]), s[11]); b = fmaxf(fmaxf(b, s[12]), s[13]); a = fmaxf(fmaxf(a, s[14]), s[15]);
    const float m = fmaxf(a, b); auto rr = __builtin_amdgcn_permlane32_swap(__float_as_uint(m), __float_as_uint(m), false, false);
    return fmaxf(__uint_as_float(rr[0]), __uint_as_float(rr[1]));
}
DI f32x16 splat16(float v) { f32x16 r;
#pragma unroll
    for (int i = 0; i < 16; ++i) r[i] = v; return r; }
DI void exp_pack(f32x16& s, bf16x8& p0, bf16x8& p1) {
#pragma unroll
    for (int i = 0; i < 16; ++i) s[i] = __builtin_amdgcn_exp2f(s[i]);
    p0 = __builtin_bit_cast(bf16x8, pack8(s, 0, 1.f)); p1 = __builtin_bit_cast(bf16x8, pack8(s, 8, 1.f));
}
DI bf16x8 vfix(bf16x8 v, bool lo) { const bf16x8 ones = {0x3F80, 0x3F80, 0x3F80, 0x3F80, 0x3F80, 0x3F80, 0x3F80, 0x3F80}; return lo ? v : ones; }

__global__ __launch_bounds__(512, 2) void fused_kernel(const uint16_t* xA, const uint16_t* wA, const uint16_t* woA, const uint16_t* xB, const uint16_t* wB, const uint16_t* woB, char* ws, uint16_t* out) {
    extern __shared__ __attribute__((aligned(16))) char smem[];
    const int bid = blockIdx.x, xc = bid & 7, j = bid >> 3;
    const int lane = threadIdx.x & 63, wv = __builtin_amdgcn_readfirstlane(threadIdx.x >> 6), hi = lane >> 5, l31 = lane & 31;
    const unsigned lds0 = (unsigned)(uintptr_t)(LDSP(char))smem;
    const u64 tag = *(const u64*)ws + 1;
    const rsrc_t rs = __builtin_amdgcn_make_buffer_rsrc(ws, 0, WS_BYTES, 0x00020000);
    RSTAMP(0);
    const int rb = (xc >> 1) * 8 + (j & 7), hg = (xc & 1) * 4 + (j >> 3), c0 = ((j & 7) + 2 * (j >> 3)) & 7;
    const int h = xc * 4 + (j & 3), qb = j >> 2;
    if (wv >= 4) {
        const int lw = wv - 4;
        unsigned voffx[2], voffw[3];
#pragma unroll
        for (int m = 0; m < 2; ++m) voffx[m] = (8 * (2 * lw + m) + (lane >> 3)) * 1024 + (((lane & 7) ^ ((4 * m + (lane >> 4)) & 7)) << 4);
#pragma unroll
        for (int t = 0; t < 3; ++t) { const int off = t * 1024 + lane * 16, r8 = off / 384, c = (off % 384) >> 4; voffw[t] = r8 * 3072 + (c >> 3) * 1024 + hg * 128 + (((c & 7) ^ (((r8 >> 1) & 1) << 2)) << 4); }
        auto stage = [&](int g) {
            const int p = g >> 3, kc = (c0 + g) & 7;
            const char* xb = (const char*)(p ? xB : xA) + (size_t)rb * 64 * 1024 + kc * 128;
            const char* wb = (const char*)(p ? wB : wA) + (size_t)(kc * 64 + 16 * lw) * 3072;
            const unsigned d = lds0 + (g & 3) * STAGE;
#pragma unroll
            for (int m = 0; m < 2; ++m) glds16<0>(xb, voffx[m], d + (2 * lw + m) * 1024);
#pragma unroll
            for (int t = 0; t < 6; ++t) glds16<0>(wb + (t / 3) * 8 * 3072, voffw[t % 3], d + 8192 + (6 * lw + t) * 1024);
        };
        auto kvstage = [&](int s) {
            const char* src = ws + WS_KV + (size_t)h * KV_HEAD + s * STAGE + lw * 8192;
#pragma unroll
            for (int i = 0; i < 8; ++i) glds16<1>(src + i * 1024, lane * 16, lds0 + s * STAGE + lw * 8192 + i * 1024);
        };
        stage(0); stage(1);
        WAIT_VM(8); BARRIER();
        stage(2); stage(3);
        WAIT_VM(16); BARRIER();
#pragma unroll
        for (int g = 2; g < 16; ++g) {
            if (g + 2 <= 15) stage(g + 2);
            if (g <= 13) WAIT_VM(16); else if (g == 14) WAIT_VM(8); else WAIT_VM(0);
            RSTAMP(2 + g);
            BARRIER();
        }
        BARRIER();
        wait_flags(ws, WS_FQ + (((h >> 1) * 64) + lane) * 8, true, tag);
        kvstage(0); kvstage(1); kvstage(2); kvstage(3);
        WAIT_VM(0); RSTAMP(18);
        BARRIER();
        {
            const int qw = qb * 256 + 64 * lw, T0 = qw / 32 - 8; const bool vlo = l31 < 16;
            { const int t = T0 + lane; wait_flags(ws, WS_FQ + ((16 + (h >> 1)) * 64 + t) * 8, lane < 10 && t >= 0, tag); }
            RSTAMP(19);
            const unsigned kvb = WS_KV + KV_BR + h * KV_HEAD, kofs = kvb + lane * 16, vofs = kvb + 1024 + hi * 256 + (l31 & 15) * 16;
            bf16x8 kf[10];
#pragma unroll
            for (int t = 0; t < 10; ++t) { const int g = T0 + t < 0 ? 0 : T0 + t; kf[t] = ld_sc1(rs, kofs + g * 2048); }
            const unsigned qofs = WS_Q + Q_BR + h * Q_HEAD + (qw / 32) * 1024 + lane * 16;
            const bf16x8 q0f = ld_sc1(rs, qofs), q1f = ld_sc1(rs, qofs + 1024);
            f32x16 mF, mL;
#pragma unroll
            for (int r = 0; r < 16; ++r) { const int jj = (r & 3) + 8 * (r >> 2) + 4 * hi; mF[r] = jj > l31 ? 0.f : -INFINITY; mL[r] = jj <= l31 ? 0.f : -INFINITY; }
#pragma unroll
            for (int blk = 0; blk < 2; ++blk) {
                const bf16x8 qf = blk ? q1f : q0f;
                const int tmin = (T0 + blk < 0) ? -(T0 + blk) : 0;
                bf16x8 v0[9], v1[9];
#pragma unroll
                for (int t = 0; t < 9; ++t) { const int g = T0 + blk + t < 0 ? 0 : T0 + blk + t; v0[t] = ld_sc1(rs, vofs + g * 2048); v1[t] = ld_sc1(rs, vofs + g * 2048 + 512); }
                float m = rowmax16(MFMA(kf[blk + 8], qf, mL));
#pragma unroll
                for (int t = 1; t < 8; ++t) if (t >= tmin) m = fmaxf(m, rowmax16(MFMA(kf[blk + t], qf, splat16(0.f))));
                if (tmin == 0) m = fmaxf(m, rowmax16(MFMA(kf[blk], qf, mF)));
                f32x16 o = {}; const f32x16 cm = splat16(-m);
#pragma unroll
                for (int t = 0; t <= 8; ++t) if (t >= tmin) {
                    f32x16 c = cm;
                    if (t == 0) {
#pragma unroll
                        for (int r = 0; r < 16; ++r) c[r] += mF[r]; }
                    if (t == 8) {
#pragma unroll
                        for (int r = 0; r < 16; ++r) c[r] += mL[r]; }
                    f32x16 s = MFMA(kf[blk + t], qf, c); bf16x8 p0, p1; exp_pack(s, p0, p1);
                    o = MFMA(vfix(v0[t], vlo), p0, o); o = MFMA(vfix(v1[t], vlo), p1, o);
                }
                st_sc1(rs, WS_O + Q_BR + h * Q_HEAD + (qw / 32 + blk) * 1024 + lane * 16, pack8(o, 0, 1.f / o[8]));
            }
            WAIT_VM(0);
            if (lane == 0) flag_st(ws, WS_FO + ((32 + qw / 64) * 32 + h) * 8, tag);
            RSTAMP(20);
        }
    } else {
        const int tt = wv & 1, hp = wv >> 1, q = (lane >> 2) & 3;
        {
            const unsigned sp = lds0 + LDS_SPARE + wv * 1024;
            const int p = wv >> 1;
            if ((wv & 1) == 0) { const char* xb = (const char*)(p ? xB : xA) + (size_t)(xc >> 1) * 512 * 1024 + j * 16384; touch(xb, lane * 128, sp); touch(xb + 8192, lane * 128, sp + 256);
                                 const char* wo = (const char*)(p ? woB : woA) + bid * 2048; if (lane < 16) touch(wo, lane * 128, sp + 512); }
            else { const char* wb = (const char*)(p ? wB : wA) + (xc & 1) * 512;
#pragma unroll
                   for (int i = 0; i < 3; ++i) { const int li = j * 192 + i * 64 + lane, k = li / 12, r = li % 12; touch(wb, k * 3072 + (r >> 2) * 1024 + (r & 3) * 128, sp + i * 256); } }
        }
        const int xrow = 32 * tt + l31; const unsigned xaddr0 = xrow * 128 + ((hi ^ ((l31 >> 1) & 7)) << 4);
        const unsigned wbase = 8192 + (8 * hi + q) * 384 + ((hp ^ (q >> 1)) * 64) + ((lane >> 4) & 1) * 32 + (lane & 3) * 8;
        f32x16 acc0, acc1, acc2, fx, fw0, fw1, fw2;
        unsigned xb0 = lds0 + xaddr0, xb1 = lds0 + (xaddr0 ^ 32), xb2 = lds0 + (xaddr0 ^ 64), xb3 = lds0 + (xaddr0 ^ 96), wb_ = lds0 + wbase;
        unsigned soff = 0;
        asm volatile(QKV_FIRST_ASM : "={v[48:63]}"(fx), "={v[64:79]}"(fw0), "={v[80:95]}"(fw1), "={v[96:111]}"(fw2)
                     : [soff] "s"(soff), "{v176}"(xb0), "{v177}"(xb1), "{v178}"(xb2), "{v179}"(xb3), "{v180}"(wb_) : "v181", "v182", "v183", "v184", "v185", "memory");
        RSTAMP(1);
#define QKV_RUN(N) do { int cnt = (N); asm volatile(QKV_PHASE_ASM : "+{v[0:15]}"(acc0), "+{v[16:31]}"(acc1), "+{v[32:47]}"(acc2), "+{v[48:63]}"(fx), "+{v[64:79]}"(fw0), "+{v[80:95]}"(fw1), "+{v[96:111]}"(fw2), [soff] "+s"(soff), [cnt] "+s"(cnt) \
                         : "{v176}"(xb0), "{v177}"(xb1), "{v178}"(xb2), "{v179}"(xb3), "{v180}"(wb_) : QKV_CLOB_B, "scc", "memory"); } while (0)
        const int tile = rb * 2 + tt, h0 = 4 * hg + 2 * hp;
        auto epilogue = [&](int br) {
            const unsigned qo = WS_Q + br * Q_BR + h0 * Q_HEAD + tile * 1024 + lane * 16, ko = WS_KV + br * KV_BR + h0 * KV_HEAD + tile * 2048 + lane * 16;
            st_sc1(rs, ko, pack8(acc1, 0, 1.f)); st_sc1(rs, ko + KV_HEAD, pack8(acc1, 8, 1.f));
            const unsigned vo = WS_KV + br * KV_BR + (h0 + (l31 >> 4)) * KV_HEAD + tile * 2048 + 1024 + hi * 256 + (l31 & 15) * 16;
            st_sc1(rs, vo, pack8(acc2, 0, 1.f)); st_sc1(rs, vo + 512, pack8(acc2, 8, 1.f));
            st_sc1(rs, qo, pack8(acc0, 0, QSCALE)); st_sc1(rs, qo + Q_HEAD, pack8(acc0, 8, QSCALE));
        };
        acc0 = f32x16{}; acc1 = f32x16{}; acc2 = f32x16{};
        QKV_RUN(4);
        RSTAMP(2);
        epilogue(0);
        acc0 = f32x16{}; acc1 = f32x16{}; acc2 = f32x16{};
        QKV_RUN(1);
        WAIT_VM(0);
        if (lane == 0) flag_st(ws, WS_FQ + (((h0 >> 1) * 64) + tile) * 8, tag);
        RSTAMP(3);
        QKV_RUN(3);
        RSTAMP(4);
        epilogue(1);
        BARRIER();
        RSTAMP(5);
        {
            const int qw = qb * 256 + 64 * wv;
            const unsigned qofs = WS_Q + h * Q_HEAD + (qw / 32) * 1024 + lane * 16;
            bf16x8 qa = ld_sc1(rs, qofs), qbf = ld_sc1(rs, qofs + 1024);
            __builtin_amdgcn_s_setprio(2);
            const bf16x8 k0 = lds_b128((lds_cptr)smem + lane * 16);
            f32x16 ca = splat16(-rowmax16(MFMA(k0, qa, splat16(0.f)))), cb = splat16(-rowmax16(MFMA(k0, qbf, splat16(0.f))));
            unsigned kaddr = lds0 + lane * 16, vaddr = lds0 + 1024 + hi * 256 + (l31 & 15) * 16;
            unsigned fill = (l31 == 16 || l31 == 20) ? 0x3f803f80u : 0u;
            unsigned foff = WS_FQ + ((16 + (h0 >> 1)) * 64 + tile) * 8;
            f32x16 oa = {}, ob = {};
            int cnt = 16; const u64 vmask = 0x0000FFFF0000FFFFull;
            RSTAMP(6);
            asm volatile(ATTN_A_ASM
                : "+{v[0:15]}"(oa), "+{v[16:31]}"(ob), [cnt] "+s"(cnt), "+{v136}"(kaddr), "+{v137}"(vaddr)
                : "{v[32:47]}"(ca), "{v[48:63]}"(cb), "{v[112:115]}"(qa), "{v[116:119]}"(qbf), [vmask] "s"(vmask), "{v138}"(fill), "{v139}"(foff), "{v[140:141]}"(tag), [fbase] "s"(ws)
                : ATTN_A_CLOB, "scc", "memory");
            RSTAMP(7);
            const unsigned oo = WS_O + h * Q_HEAD + (qw / 32) * 1024 + lane * 16;
            st_sc1(rs, oo, pack8(oa, 0, 1.f / oa[8])); st_sc1(rs, oo + 1024, pack8(ob, 0, 1.f / ob[8]));
            WAIT_VM(0);
            if (lane == 0) flag_st(ws, WS_FO + ((qw / 64) * 32 + h) * 8, tag);
            __builtin_amdgcn_s_setprio(0);
            RSTAMP(8);
        }
    }
    BARRIER();
    {
        const int br = xc >> 2, orb = (xc & 3) * 4 + (j & 3), cb = j >> 2, tt = wv & 3, kh = wv >> 2;
        const char* wo = (const char*)(br ? woB : woA) + cb * 128;
        const unsigned voff = (lane >> 3) * 1024 + (((lane & 7) ^ (((lane >> 4) & 1) << 2)) << 4);
#pragma unroll
        for (int i = 0; i < 8; ++i) glds16<0>(wo + (wv * 8 + i) * 8192, voff, lds0 + (wv * 8 + i) * 1024);
        const int tile = orb * 4 + tt;
        wait_flags(ws, WS_FO + ((br * 32 + (tile >> 1)) * 32 + kh * 16 + (lane & 15)) * 8, lane < 16, tag);
        RSTAMP(21);
        bf16x8 of[16];
        const unsigned oofs = WS_O + br * Q_BR + kh * 16 * Q_HEAD + tile * 1024 + lane * 16;
#pragma unroll
        for (int i = 0; i < 16; ++i) of[i] = ld_sc1(rs, oofs + i * Q_HEAD);
        const int q = (lane >> 2) & 3;
        unsigned wb[2];
#pragma unroll
        for (int ct = 0; ct < 2; ++ct) wb[ct] = (kh * 256 + 4 * hi + q) * 128 + ((ct ^ (q >> 1)) * 64) + ((lane >> 4) & 1) * 32 + (lane & 3) * 8;
        WAIT_VM(0); BARRIER();
        RSTAMP(22);
        f32x16 acc[2] = {};
#pragma unroll
        for (int i = 0; i < 16; ++i)
#pragma unroll
            for (int ct = 0; ct < 2; ++ct) { lds_cptr p = (lds_cptr)smem + wb[ct] + i * 2048; acc[ct] = MFMA(cat8(lds_tr(p), lds_tr(p + 1024)), of[i], acc[ct]); }
        LDSP(float) xs = (LDSP(float))(smem + 65536) + wv * 1024; LDSP(float) xr = (LDSP(float))(smem + 65536) + (wv ^ 4) * 1024;
#pragma unroll
        for (int r = 0; r < 16; ++r) xs[r * 64 + lane] = kh ? acc[0][r] : acc[1][r];
        BARRIER();
        f32x16 o = kh ? acc[1] : acc[0];
#pragma unroll
        for (int r = 0; r < 16; ++r) o[r] += xr[r * 64 + lane];
        uint16_t* orow = out + (size_t)(tile * 32 + l31) * 1024 + br * 512 + cb * 64 + kh * 32 + 4 * hi;
#pragma unroll
        for (int g = 0; g < 4; ++g) *(u32x2*)(orow + 8 * g) = (u32x2){cvtpk(o[4 * g], o[4 * g + 1]), cvtpk(o[4 * g + 2], o[4 * g + 3])};
        RSTAMP(23);
    }
    if (bid == 0 && wv == 7) {
        _Pragma("unroll 1") for (int i = 0; i < 16; ++i) wait_flags(ws, WS_FQ + (i * 64 + lane) * 8, true, tag);
        if (lane == 0) flag_st(ws, 0, tag);
    }
}

extern "C" void kernel_launch(void* const* d_in, const int* in_sizes, int n_in, void* d_out, int out_size, void* d_ws, size_t ws_size, hipStream_t stream) {
    (void)hipFuncSetAttribute((const void*)fused_kernel, hipFuncAttributeMaxDynamicSharedMemorySize, LDS_BYTES);
    fused_kernel<<<256, 512, LDS_BYTES, stream>>>((const uint16_t*)d_in[0], (const uint16_t*)d_in[1], (const uint16_t*)d_in[2], (const uint16_t*)d_in[3], (const uint16_t*)d_in[4], (const uint16_t*)d_in[5], (char*)d_ws, (uint16_t*)d_out);
}
```

```cpp
#include <hip/hip_runtime.h>
#include <stdint.h>
#define QKV_FIRST_ASM \
    "s_waitcnt lgkmcnt(0)\n\t" \
    "s_barrier\n\t" \
    "v_add_u32 v185, %[soff], v180\n\t" \
    "v_add_u32 v181, %[soff], v176\n\t" \
    "v_add_u32 v182, %[soff], v177\n\t" \
    "v_add_u32 v183, %[soff], v178\n\t" \
    "v_add_u32 v184, %[soff], v179\n\t" \
    "ds_read_b128 v[48:51], v181\n\t" \
    "ds_read_b64_tr_b16 v[64:65], v185 offset:0\n\t" \
    "ds_read_b64_tr_b16 v[66:67], v185 offset:1536\n\t" \
    "ds_read_b64_tr_b16 v[68:69], v185 offset:128\n\t" \
    "ds_read_b64_tr_b16 v[70:71], v185 offset:1664\n\t" \
    "ds_read_b64_tr_b16 v[72:73], v185 offset:256\n\t" \
    "ds_read_b64_tr_b16 v[74:75], v185 offset:1792\n\t" \
    "ds_read_b128 v[52:55], v182\n\t" \
    "ds_read_b64_tr_b16 v[76:77], v185 offset:6144\n\t" \
    "ds_read_b64_tr_b16 v[78:79], v185 offset:7680\n\t" \
    "ds_read_b64_tr_b16 v[80:81], v185 offset:6272\n\t" \
    "ds_read_b64_tr_b16 v[82:83], v185 offset:7808\n\t" \
    "ds_read_b64_tr_b16 v[84:85], v185 offset:6400\n\t" \
    "ds_read_b64_tr_b16 v[86:87], v185 offset:7936\n\t" \
    "ds_read_b128 v[56:59], v183\n\t" \
    "ds_read_b64_tr_b16 v[88:89], v185 offset:12288\n\t" \
    "ds_read_b64_tr_b16 v[90:91], v185 offset:13824\n\t" \
    "ds_read_b64_tr_b16 v[92:93], v185 offset:12416\n\t" \
    "ds_read_b64_tr_b16 v[94:95], v185 offset:13952\n\t" \
    "ds_read_b64_tr_b16 v[96:97], v185 offset:12544\n\t" \
    "ds_read_b64_tr_b16 v[98:99], v185 offset:14080\n\t" \
    "ds_read_b128 v[60:63], v184\n\t" \
    "ds_read_b64_tr_b16 v[100:101], v185 offset:18432\n\t" \
    "ds_read_b64_tr_b16 v[102:103], v185 offset:19968\n\t" \
    "ds_read_b64_tr_b16 v[104:105], v185 offset:18560\n\t" \
    "ds_read_b64_tr_b16 v[106:107], v185 offset:20096\n\t" \
    "ds_read_b64_tr_b16 v[108:109], v185 offset:18688\n\t" \
    "ds_read_b64_tr_b16 v[110:111], v185 offset:20224\n\t" \
    "s_waitcnt lgkmcnt(0)\n\t"

#define QKV_PHASE_ASM \
    "1:\n\t" \
    "s_waitcnt lgkmcnt(0)\n\t" \
    "s_barrier\n\t" \
    "v_mfma_f32_32x32x16_bf16 v[0:15], v[64:67], v[48:51], v[0:15]\n\t" \
    "s_add_u32 %[soff], %[soff], 0x8000\n\t" \
    "s_and_b32 %[soff], %[soff], 0x18000\n\t" \
    "v_add_u32 v185, %[soff], v180\n\t" \
    "v_add_u32 v181, %[soff], v176\n\t" \
    "v_add_u32 v182, %[soff], v177\n\t" \
    "v_add_u32 v183, %[soff], v178\n\t" \
    "v_add_u32 v184, %[soff], v179\n\t" \
    "ds_read_b128 v[112:115], v181\n\t" \
    "ds_read_b64_tr_b16 v[128:129], v185 offset:0\n\t" \
    "ds_read_b64_tr_b16 v[130:131], v185 offset:1536\n\t" \
    "v_mfma_f32_32x32x16_bf16 v[16:31], v[68:71], v[48:51], v[16:31]\n\t" \
    "ds_read_b64_tr_b16 v[132:133], v185 offset:128\n\t" \
    "ds_read_b64_tr_b16 v[134:135], v185 offset:1664\n\t" \
    "ds_read_b64_tr_b16 v[136:137], v185 offset:256\n\t" \
    "v_mfma_f32_32x32x16_bf16 v[32:47], v[48:51], v[72:75], v[32:47]\n\t" \
    "ds_read_b64_tr_b16 v[138:139], v185 offset:1792\n\t" \
    "ds_read_b128 v[116:119], v182\n\t" \
    "ds_read_b64_tr_b16 v[140:141], v185 offset:6144\n\t" \
    "v_mfma_f32_32x32x16_bf16 v[0:15], v[76:79], v[52:55], v[0:15]\n\t" \
    "ds_read_b64_tr_b16 v[142:143], v185 offset:7680\n\t" \
    "ds_read_b64_tr_b16 v[144:145], v185 offset:6272\n\t" \
    "ds_read_b64_tr_b16 v[146:147], v185 offset:7808\n\t" \
    "v_mfma_f32_32x32x16_bf16 v[16:31], v[80:83], v[52:55], v[16:31]\n\t" \
    "ds_read_b64_tr_b16 v[148:149], v185 offset:6400\n\t" \
    "ds_read_b64_tr_b16 v[150:151], v185 offset:7936\n\t" \
    "ds_read_b128 v[120:123], v183\n\t" \
    "v_mfma_f32_32x32x16_bf16 v[32:47], v[52:55], v[84:87], v[32:47]\n\t" \
    "ds_read_b64_tr_b16 v[152:153], v185 offset:12288\n\t" \
    "ds_read_b64_tr_b16 v[154:155], v185 offset:13824\n\t" \
    "ds_read_b64_tr_b16 v[156:157], v185 offset:12416\n\t" \
    "v_mfma_f32_32x32x16_bf16 v[0:15], v[88:91], v[56:59], v[0:15]\n\t" \
    "ds_read_b64_tr_b16 v[158:159], v185 offset:13952\n\t" \
    "ds_read_b64_tr_b16 v[160:161], v185 offset:12544\n\t" \
    "ds_read_b64_tr_b16 v[162:163], v185 offset:14080\n\t" \
    "v_mfma_f32_32x32x16_bf16 v[16:31], v[92:95], v[56:59], v[16:31]\n\t" \
    "ds_read_b128 v[124:127], v184\n\t" \
    "ds_read_b64_tr_b16 v[164:165], v185 offset:18432\n\t" \
    "v_mfma_f32_32x32x16_bf16 v[32:47], v[56:59], v[96:99], v[32:47]\n\t" \
    "ds_read_b64_tr_b16 v[166:167], v185 offset:19968\n\t" \
    "ds_read_b64_tr_b16 v[168:169], v185 offset:18560\n\t" \
    "v_mfma_f32_32x32x16_bf16 v[0:15], v[100:103], v[60:63], v[0:15]\n\t" \
    "ds_read_b64_tr_b16 v[170:171], v185 offset:20096\n\t" \
    "ds_read_b64_tr_b16 v[172:173], v185 offset:18688\n\t" \
    "v_mfma_f32_32x32x16_bf16 v[16:31], v[104:107], v[60:63], v[16:31]\n\t" \
    "ds_read_b64_tr_b16 v[174:175], v185 offset:20224\n\t" \
    "v_mfma_f32_32x32x16_bf16 v[32:47], v[60:63], v[108:111], v[32:47]\n\t" \
    "s_waitcnt lgkmcnt(0)\n\t" \
    "s_barrier\n\t" \
    "v_mfma_f32_32x32x16_bf16 v[0:15], v[128:131], v[112:115], v[0:15]\n\t" \
    "s_add_u32 %[soff], %[soff], 0x8000\n\t" \
    "s_and_b32 %[soff], %[soff], 0x18000\n\t" \
    "v_add_u32 v185, %[soff], v180\n\t" \
    "v_add_u32 v181, %[soff], v176\n\t" \
    "v_add_u32 v182, %[soff], v177\n\t" \
    "v_add_u32 v183, %[soff], v178\n\t" \
    "v_add_u32 v184, %[soff], v179\n\t" \
    "ds_read_b128 v[48:51], v181\n\t" \
    "ds_read_b64_tr_b16 v[64:65], v185 offset:0\n\t" \
    "ds_read_b64_tr_b16 v[66:67], v185 offset:1536\n\t" \
    "v_mfma_f32_32x32x16_bf16 v[16:31], v[132:135], v[112:115], v[16:31]\n\t" \
    "ds_read_b64_tr_b16 v[68:69], v185 offset:128\n\t" \
    "ds_read_b64_tr_b16 v[70:71], v185 offset:1664\n\t" \
    "ds_read_b64_tr_b16 v[72:73], v185 offset:256\n\t" \
    "v_mfma_f32_32x32x16_bf16 v[32:47], v[112:115], v[136:139], v[32:47]\n\t" \
    "ds_read_b64_tr_b16 v[74:75], v185 offset:1792\n\t" \
    "ds_read_b128 v[52:55], v182\n\t" \
    "ds_read_b64_tr_b16 v[76:77], v185 offset:6144\n\t" \
    "v_mfma_f32_32x32x16_bf16 v[0:15], v[140:143], v[116:119], v[0:15]\n\t" \
    "ds_read_b64_tr_b16 v[78:79], v185 offset:7680\n\t" \
    "ds_read_b64_tr_b16 v[80:81], v185 offset:6272\n\t" \
    "ds_read_b64_tr_b16 v[82:83], v185 offset:7808\n\t" \
    "v_mfma_f32_32x32x16_bf16 v[16:31], v[144:147], v[116:119], v[16:31]\n\t" \
    "ds_read_b64_tr_b16 v[84:85], v185 offset:6400\n\t" \
    "ds_read_b64_tr_b16 v[86:87], v185 offset:7936\n\t" \
    "ds_read_b128 v[56:59], v183\n\t" \
    "v_mfma_f32_32x32x16_bf16 v[32:47], v[116:119], v[148:151], v[32:47]\n\t" \
    "ds_read_b64_tr_b16 v[88:89], v185 offset:12288\n\t" \
    "ds_read_b64_tr_b16 v[90:91], v185 offset:13824\n\t" \
    "ds_read_b64_tr_b16 v[92:93], v185 offset:12416\n\t" \
    "v_mfma_f32_32x32x16_bf16 v[0:15], v[152:155], v[120:123], v[0:15]\n\t" \
    "ds_read_b64_tr_b16 v[94:95], v185 offset:13952\n\t" \
    "ds_read_b64_tr_b16 v[96:97], v185 offset:12544\n\t" \
    "ds_read_b64_tr_b16 v[98:99], v185 offset:14080\n\t" \
    "v_mfma_f32_32x32x16_bf16 v[16:31], v[156:159], v[120:123], v[16:31]\n\t" \
    "ds_read_b128 v[60:63], v184\n\t" \
    "ds_read_b64_tr_b16 v[100:101], v185 offset:18432\n\t" \
    "v_mfma_f32_32x32x16_bf16 v[32:47], v[120:123], v[160:163], v[32:47]\n\t" \
    "ds_read_b64_tr_b16 v[102:103], v185 offset:19968\n\t" \
    "ds_read_b64_tr_b16 v[104:105], v185 offset:18560\n\t" \
    "v_mfma_f32_32x32x16_bf16 v[0:15], v[164:167], v[124:127], v[0:15]\n\t" \
    "ds_read_b64_tr_b16 v[106:107], v185 offset:20096\n\t" \
    "ds_read_b64_tr_b16 v[108:109], v185 offset:18688\n\t" \
    "v_mfma_f32_32x32x16_bf16 v[16:31], v[168:171], v[124:127], v[16:31]\n\t" \
    "ds_read_b64_tr_b16 v[110:111], v185 offset:20224\n\t" \
    "v_mfma_f32_32x32x16_bf16 v[32:47], v[124:127], v[172:175], v[32:47]\n\t" \
    "s_sub_u32 %[cnt], %[cnt], 1\n\t" \
    "s_cmp_lg_u32 %[cnt], 0\n\t" \
    "s_cbranch_scc1 1b\n\t" \
    "s_waitcnt lgkmcnt(0)\n\t" \
    "s_nop 15\n\t"

#define QKV_CLOB_B "v112","v113","v114","v115","v116","v117","v118","v119","v120","v121","v122","v123","v124","v125","v126","v127","v128","v129","v130","v131","v132","v133","v134","v135","v136","v137","v138","v139","v140","v141","v142","v143","v144","v145","v146","v147","v148","v149","v150","v151","v152","v153","v154","v155","v156","v157","v158","v159","v160","v161","v162","v163","v164","v165","v166","v167","v168","v169","v170","v171","v172","v173","v174","v175","v181","v182","v183","v184","v185"

#define ATTN_A_ASM \
    "v_mov_b32 v124, v138\n\t" \
    "v_mov_b32 v125, v138\n\t" \
    "v_mov_b32 v126, v138\n\t" \
    "v_mov_b32 v127, v138\n\t" \
    "v_mov_b32 v128, v138\n\t" \
    "v_mov_b32 v129, v138\n\t" \
    "v_mov_b32 v130, v138\n\t" \
    "v_mov_b32 v131, v138\n\t" \
    "v_mov_b32 v132, v138\n\t" \
    "v_mov_b32 v133, v138\n\t" \
    "v_mov_b32 v134, v138\n\t" \
    "v_mov_b32 v135, v138\n\t" \
    "v_mov_b32 v108, 0\n\t" \
    "v_mov_b32 v109, 0\n\t" \
    "v_mov_b32 v110, 0\n\t" \
    "v_mov_b32 v111, 0\n\t" \
    "ds_read_b128 v[120:123], v136\n\t" \
    "s_mov_b64 exec, %[vmask]\n\t" \
    "ds_read_b128 v[124:127], v137\n\t" \
    "ds_read_b128 v[128:131], v137 offset:512\n\t" \
    "s_mov_b64 exec, -1\n\t" \
    "s_waitcnt lgkmcnt(2)\n\t" \
    "v_mfma_f32_32x32x16_bf16 v[64:79], v[120:123], v[112:115], v[32:47]\n\t" \
    "s_nop 15\n\t" \
    "1:\n\t" \
    "v_exp_f32 v64, v64\n\t" \
    "v_exp_f32 v65, v65\n\t" \
    "v_exp_f32 v66, v66\n\t" \
    "v_cvt_pk_bf16_f32 v96, v64, v65\n\t" \
    "v_exp_f32 v67, v67\n\t" \
    "v_exp_f32 v68, v68\n\t" \
    "v_cvt_pk_bf16_f32 v97, v66, v67\n\t" \
    "v_exp_f32 v69, v69\n\t" \
    "v_mfma_f32_32x32x16_bf16 v[80:95], v[120:123], v[116:119], v[48:63]\n\t" \
    "ds_read_b128 v[120:123], v136 offset:2048\n\t" \
    "v_exp_f32 v70, v70\n\t" \
    "v_cvt_pk_bf16_f32 v98, v68, v69\n\t" \
    "v_exp_f32 v71, v71\n\t" \
    "v_exp_f32 v72, v72\n\t" \
    "v_cvt_pk_bf16_f32 v99, v70, v71\n\t" \
    "v_exp_f32 v73, v73\n\t" \
    "v_exp_f32 v74, v74\n\t" \
    "v_cvt_pk_bf16_f32 v100, v72, v73\n\t" \
    "s_waitcnt lgkmcnt(2)\n\t" \
    "v_mfma_f32_32x32x16_bf16 v[0:15], v[124:127], v[96:99], v[0:15]\n\t" \
    "v_exp_f32 v75, v75\n\t" \
    "v_exp_f32 v76, v76\n\t" \
    "v_cvt_pk_bf16_f32 v101, v74, v75\n\t" \
    "v_exp_f32 v77, v77\n\t" \
    "v_exp_f32 v78, v78\n\t" \
    "v_exp_f32 v79, v79\n\t" \
    "v_cvt_pk_bf16_f32 v102, v76, v77\n\t" \
    "v_cvt_pk_bf16_f32 v103, v78, v79\n\t" \
    "v_mfma_f32_32x32x16_bf16 v[16:31], v[132:135], v[108:111], v[16:31]\n\t" \
    "v_exp_f32 v80, v80\n\t" \
    "v_exp_f32 v81, v81\n\t" \
    "v_exp_f32 v82, v82\n\t" \
    "v_cvt_pk_bf16_f32 v104, v80, v81\n\t" \
    "v_exp_f32 v83, v83\n\t" \
    "v_exp_f32 v84, v84\n\t" \
    "v_cvt_pk_bf16_f32 v105, v82, v83\n\t" \
    "v_exp_f32 v85, v85\n\t" \
    "s_waitcnt lgkmcnt(0)\n\t" \
    "v_mfma_f32_32x32x16_bf16 v[64:79], v[120:123], v[112:115], v[32:47]\n\t" \
    "v_exp_f32 v86, v86\n\t" \
    "v_cvt_pk_bf16_f32 v106, v84, v85\n\t" \
    "v_exp_f32 v87, v87\n\t" \
    "v_exp_f32 v88, v88\n\t" \
    "v_cvt_pk_bf16_f32 v107, v86, v87\n\t" \
    "v_exp_f32 v89, v89\n\t" \
    "v_exp_f32 v90, v90\n\t" \
    "v_cvt_pk_bf16_f32 v108, v88, v89\n\t" \
    "v_mfma_f32_32x32x16_bf16 v[16:31], v[124:127], v[104:107], v[16:31]\n\t" \
    "s_mov_b64 exec, %[vmask]\n\t" \
    "ds_read_b128 v[124:127], v137 offset:2048\n\t" \
    "ds_read_b128 v[132:135], v137 offset:2560\n\t" \
    "s_mov_b64 exec, -1\n\t" \
    "v_exp_f32 v91, v91\n\t" \
    "v_exp_f32 v92, v92\n\t" \
    "v_cvt_pk_bf16_f32 v109, v90, v91\n\t" \
    "v_exp_f32 v93, v93\n\t" \
    "v_exp_f32 v94, v94\n\t" \
    "v_exp_f32 v95, v95\n\t" \
    "v_cvt_pk_bf16_f32 v110, v92, v93\n\t" \
    "v_cvt_pk_bf16_f32 v111, v94, v95\n\t" \
    "v_mfma_f32_32x32x16_bf16 v[0:15], v[128:131], v[100:103], v[0:15]\n\t" \
    "v_exp_f32 v64, v64\n\t" \
    "v_exp_f32 v65, v65\n\t" \
    "v_exp_f32 v66, v66\n\t" \
    "v_cvt_pk_bf16_f32 v96, v64, v65\n\t" \
    "v_exp_f32 v67, v67\n\t" \
    "v_exp_f32 v68, v68\n\t" \
    "v_cvt_pk_bf16_f32 v97, v66, v67\n\t" \
    "v_exp_f32 v69, v69\n\t" \
    "v_mfma_f32_32x32x16_bf16 v[80:95], v[120:123], v[116:119], v[48:63]\n\t" \
    "ds_read_b128 v[120:123], v136 offset:4096\n\t" \
    "v_exp_f32 v70, v70\n\t" \
    "v_cvt_pk_bf16_f32 v98, v68, v69\n\t" \
    "v_exp_f32 v71, v71\n\t" \
    "v_exp_f32 v72, v72\n\t" \
    "v_cvt_pk_bf16_f32 v99, v70, v71\n\t" \
    "v_exp_f32 v73, v73\n\t" \
    "v_exp_f32 v74, v74\n\t" \
    "v_cvt_pk_bf16_f32 v100, v72, v73\n\t" \
    "s_waitcnt lgkmcnt(2)\n\t" \
    "v_mfma_f32_32x32x16_bf16 v[0:15], v[124:127], v[96:99], v[0:15]\n\t" \
    "v_exp_f32 v75, v75\n\t" \
    "v_exp_f32 v76, v76\n\t" \
    "v_cvt_pk_bf16_f32 v101, v74, v75\n\t" \
    "v_exp_f32 v77, v77\n\t" \
    "v_exp_f32 v78, v78\n\t" \
    "v_exp_f32 v79, v79\n\t" \
    "v_cvt_pk_bf16_f32 v102, v76, v77\n\t" \
    "v_cvt_pk_bf16_f32 v103, v78, v79\n\t" \
    "v_mfma_f32_32x32x16_bf16 v[16:31], v[128:131], v[108:111], v[16:31]\n\t" \
    "v_exp_f32 v80, v80\n\t" \
    "v_exp_f32 v81, v81\n\t" \
    "v_exp_f32 v82, v82\n\t" \
    "v_cvt_pk_bf16_f32 v104, v80, v81\n\t" \
    "v_exp_f32 v83, v83\n\t" \
    "v_exp_f32 v84, v84\n\t" \
    "v_cvt_pk_bf16_f32 v105, v82, v83\n\t" \
    "v_exp_f32 v85, v85\n\t" \
    "s_waitcnt lgkmcnt(0)\n\t" \
    "v_mfma_f32_32x32x16_bf16 v[64:79], v[120:123], v[112:115], v[32:47]\n\t" \
    "v_exp_f32 v86, v86\n\t" \
    "v_cvt_pk_bf16_f32 v106, v84, v85\n\t" \
    "v_exp_f32 v87, v87\n\t" \
    "v_exp_f32 v88, v88\n\t" \
    "v_cvt_pk_bf16_f32 v107, v86, v87\n\t" \
    "v_exp_f32 v89, v89\n\t" \
    "v_exp_f32 v90, v90\n\t" \
    "v_cvt_pk_bf16_f32 v108, v88, v89\n\t" \
    "v_mfma_f32_32x32x16_bf16 v[16:31], v[124:127], v[104:107], v[16:31]\n\t" \
    "s_mov_b64 exec, %[vmask]\n\t" \
    "ds_read_b128 v[124:127], v137 offset:4096\n\t" \
    "ds_read_b128 v[128:131], v137 offset:4608\n\t" \
    "s_mov_b64 exec, -1\n\t" \
    "v_exp_f32 v91, v91\n\t" \
    "v_exp_f32 v92, v92\n\t" \
    "v_cvt_pk_bf16_f32 v109, v90, v91\n\t" \
    "v_exp_f32 v93, v93\n\t" \
    "v_exp_f32 v94, v94\n\t" \
    "v_exp_f32 v95, v95\n\t" \
    "v_cvt_pk_bf16_f32 v110, v92, v93\n\t" \
    "v_cvt_pk_bf16_f32 v111, v94, v95\n\t" \
    "v_mfma_f32_32x32x16_bf16 v[0:15], v[132:135], v[100:103], v[0:15]\n\t" \
    "v_exp_f32 v64, v64\n\t" \
    "v_exp_f32 v65, v65\n\t" \
    "v_exp_f32 v66, v66\n\t" \
    "v_cvt_pk_bf16_f32 v96, v64, v65\n\t" \
    "v_exp_f32 v67, v67\n\t" \
    "v_exp_f32 v68, v68\n\t" \
    "v_cvt_pk_bf16_f32 v97, v66, v67\n\t" \
    "v_exp_f32 v69, v69\n\t" \
    "v_mfma_f32_32x32x16_bf16 v[80:95], v[120:123], v[116:119], v[48:63]\n\t" \
    "ds_read_b128 v[120:123], v136 offset:6144\n\t" \
    "v_exp_f32 v70, v70\n\t" \
    "v_cvt_pk_bf16_f32 v98, v68, v69\n\t" \
    "v_exp_f32 v71, v71\n\t" \
    "v_exp_f32 v72, v72\n\t" \
    "v_cvt_pk_bf16_f32 v99, v70, v71\n\t" \
    "v_exp_f32 v73, v73\n\t" \
    "v_exp_f32 v74, v74\n\t" \
    "v_cvt_pk_bf16_f32 v100, v72, v73\n\t" \
    "s_waitcnt lgkmcnt(2)\n\t" \
    "v_mfma_f32_32x32x16_bf16 v[0:15], v[124:127], v[96:99], v[0:15]\n\t" \
    "v_exp_f32 v75, v75\n\t" \
    "v_exp_f32 v76, v76\n\t" \
    "v_cvt_pk_bf16_f32 v101, v74, v75\n\t" \
    "v_exp_f32 v77, v77\n\t" \
    "v_exp_f32 v78, v78\n\t" \
    "v_exp_f32 v79, v79\n\t" \
    "v_cvt_pk_bf16_f32 v102, v76, v77\n\t" \
    "v_cvt_pk_bf16_f32 v103, v78, v79\n\t" \
    "v_mfma_f32_32x32x16_bf16 v[16:31], v[132:135], v[108:111], v[16:31]\n\t" \
    "v_exp_f32 v80, v80\n\t" \
    "v_exp_f32 v81, v81\n\t" \
    "v_exp_f32 v82, v82\n\t" \
    "v_cvt_pk_bf16_f32 v104, v80, v81\n\t" \
    "v_exp_f32 v83, v83\n\t" \
    "v_exp_f32 v84, v84\n\t" \
    "v_cvt_pk_bf16_f32 v105, v82, v83\n\t" \
    "v_exp_f32 v85, v85\n\t" \
    "s_waitcnt lgkmcnt(0)\n\t" \
    "v_mfma_f32_32x32x16_bf16 v[64:79], v[120:123], v[112:115], v[32:47]\n\t" \
    "v_exp_f32 v86, v86\n\t" \
    "v_cvt_pk_bf16_f32 v106, v84, v85\n\t" \
    "v_exp_f32 v87, v87\n\t" \
    "v_exp_f32 v88, v88\n\t" \
    "v_cvt_pk_bf16_f32 v107, v86, v87\n\t" \
    "v_exp_f32 v89, v89\n\t" \
    "v_exp_f32 v90, v90\n\t" \
    "v_cvt_pk_bf16_f32 v108, v88, v89\n\t" \
    "v_mfma_f32_32x32x16_bf16 v[16:31], v[124:127], v[104:107], v[16:31]\n\t" \
    "s_mov_b64 exec, %[vmask]\n\t" \
    "ds_read_b128 v[124:127], v137 offset:6144\n\t" \
    "ds_read_b128 v[132:135], v137 offset:6656\n\t" \
    "s_mov_b64 exec, -1\n\t" \
    "v_exp_f32 v91, v91\n\t" \
    "v_exp_f32 v92, v92\n\t" \
    "v_cvt_pk_bf16_f32 v109, v90, v91\n\t" \
    "v_exp_f32 v93, v93\n\t" \
    "v_exp_f32 v94, v94\n\t" \
    "v_exp_f32 v95, v95\n\t" \
    "v_cvt_pk_bf16_f32 v110, v92, v93\n\t" \
    "v_cvt_pk_bf16_f32 v111, v94, v95\n\t" \
    "v_mfma_f32_32x32x16_bf16 v[0:15], v[128:131], v[100:103], v[0:15]\n\t" \
    "v_exp_f32 v64, v64\n\t" \
    "v_exp_f32 v65, v65\n\t" \
    "v_exp_f32 v66, v66\n\t" \
    "v_cvt_pk_bf16_f32 v96, v64, v65\n\t" \
    "v_exp_f32 v67, v67\n\t" \
    "v_exp_f32 v68, v68\n\t" \
    "v_cvt_pk_bf16_f32 v97, v66, v67\n\t" \
    "v_exp_f32 v69, v69\n\t" \
    "v_mfma_f32_32x32x16_bf16 v[80:95], v[120:123], v[116:119], v[48:63]\n\t" \
    "ds_read_b128 v[120:123], v136 offset:8192\n\t" \
    "v_exp_f32 v70, v70\n\t" \
    "v_cvt_pk_bf16_f32 v98, v68, v69\n\t" \
    "v_exp_f32 v71, v71\n\t" \
    "v_exp_f32 v72, v72\n\t" \
    "v_cvt_pk_bf16_f32 v99, v70, v71\n\t" \
    "v_exp_f32 v73, v73\n\t" \
    "v_exp_f32 v74, v74\n\t" \
    "v_cvt_pk_bf16_f32 v100, v72, v73\n\t" \
    "s_waitcnt lgkmcnt(2)\n\t" \
    "v_mfma_f32_32x32x16_bf16 v[0:15], v[124:127], v[96:99], v[0:15]\n\t" \
    "v_exp_f32 v75, v75\n\t" \
    "v_exp_f32 v76, v76\n\t" \
    "v_cvt_pk_bf16_f32 v101, v74, v75\n\t" \
    "v_exp_f32 v77, v77\n\t" \
    "v_exp_f32 v78, v78\n\t" \
    "v_exp_f32 v79, v79\n\t" \
    "v_cvt_pk_bf16_f32 v102, v76, v77\n\t" \
    "v_cvt_pk_bf16_f32 v103, v78, v79\n\t" \
    "v_mfma_f32_32x32x16_bf16 v[16:31], v[128:131], v[108:111], v[16:31]\n\t" \
    "v_exp_f32 v80, v80\n\t" \
    "v_exp_f32 v81, v81\n\t" \
    "v_exp_f32 v82, v82\n\t" \
    "v_cvt_pk_bf16_f32 v104, v80, v81\n\t" \
    "v_exp_f32 v83, v83\n\t" \
    "v_exp_f32 v84, v84\n\t" \
    "v_cvt_pk_bf16_f32 v105, v82, v83\n\t" \
    "v_exp_f32 v85, v85\n\t" \
    "s_waitcnt lgkmcnt(0)\n\t" \
    "v_mfma_f32_32x32x16_bf16 v[64:79], v[120:123], v[112:115], v[32:47]\n\t" \
    "v_exp_f32 v86, v86\n\t" \
    "v_cvt_pk_bf16_f32 v106, v84, v85\n\t" \
    "v_exp_f32 v87, v87\n\t" \
    "v_exp_f32 v88, v88\n\t" \
    "v_cvt_pk_bf16_f32 v107, v86, v87\n\t" \
    "v_exp_f32 v89, v89\n\t" \
    "v_exp_f32 v90, v90\n\t" \
    "v_cvt_pk_bf16_f32 v108, v88, v89\n\t" \
    "v_mfma_f32_32x32x16_bf16 v[16:31], v[124:127], v[104:107], v[16:31]\n\t" \
    "s_mov_b64 exec, %[vmask]\n\t" \
    "ds_read_b128 v[124:127], v137 offset:8192\n\t" \
    "ds_read_b128 v[128:131], v137 offset:8704\n\t" \
    "s_mov_b64 exec, -1\n\t" \
    "v_exp_f32 v91, v91\n\t" \
    "v_exp_f32 v92, v92\n\t" \
    "v_cvt_pk_bf16_f32 v109, v90, v91\n\t" \
    "v_exp_f32 v93, v93\n\t" \
    "v_exp_f32 v94, v94\n\t" \
    "v_exp_f32 v95, v95\n\t" \
    "v_cvt_pk_bf16_f32 v110, v92, v93\n\t" \
    "v_cvt_pk_bf16_f32 v111, v94, v95\n\t" \
    "v_mfma_f32_32x32x16_bf16 v[0:15], v[132:135], v[100:103], v[0:15]\n\t" \
    "v_add_u32 v136, 0x2000, v136\n\t" \
    "v_add_u32 v137, 0x2000, v137\n\t" \
    "s_cmp_lg_u32 %[cnt], 16\n\t" \
    "s_cbranch_scc1 2f\n\t" \
    "s_waitcnt vmcnt(0)\n\t" \
    "s_mov_b64 exec, 1\n\t" \
    "global_store_dwordx2 v139, v[140:141], %[fbase] sc1\n\t" \
    "s_mov_b64 exec, -1\n\t" \
    "2:\n\t" \
    "s_sub_u32 %[cnt], %[cnt], 1\n\t" \
    "s_cmp_lg_u32 %[cnt], 0\n\t" \
    "s_cbranch_scc1 1b\n\t" \
    "s_nop 1\n\t" \
    "v_mfma_f32_32x32x16_bf16 v[16:31], v[132:135], v[108:111], v[16:31]\n\t" \
    "s_waitcnt lgkmcnt(0)\n\t" \
    "s_nop 15\n\t"

#define ATTN_A_CLOB "v64","v65","v66","v67","v68","v69","v70","v71","v72","v73","v74","v75","v76","v77","v78","v79","v80","v81","v82","v83","v84","v85","v86","v87","v88","v89","v90","v91","v92","v93","v94","v95","v96","v97","v98","v99","v100","v101","v102","v103","v104","v105","v106","v107","v108","v109","v110","v111","v120","v121","v122","v123","v124","v125","v126","v127","v128","v129","v130","v131","v132","v133","v134","v135"

typedef short bf16x8 __attribute__((ext_vector_type(8)));
typedef short s16x4 __attribute__((ext_vector_type(4)));
typedef float f32x16 __attribute__((ext_vector_type(16)));
typedef float f32x2 __attribute__((ext_vector_type(2)));
typedef __bf16 b16x2 __attribute__((ext_vector_type(2)));
typedef unsigned u32x4 __attribute__((ext_vector_type(4)));
typedef unsigned u32x2 __attribute__((ext_vector_type(2)));
typedef unsigned long long u64;
typedef const __attribute__((address_space(3))) char* lds_cptr;
#define LDSP(T) __attribute__((address_space(3))) T*
#define GASP(T) __attribute__((address_space(1))) T*
#define DI __device__ __forceinline__
#define MFMA(a, b, c) __builtin_amdgcn_mfma_f32_32x32x16_bf16(a, b, c, 0, 0, 0)
#define WAIT_VM(n) asm volatile("s_waitcnt vmcnt(" #n ")" ::: "memory")
#define BARRIER() asm volatile("s_waitcnt lgkmcnt(0)\n\ts_barrier" ::: "memory")

constexpr int SEQ = 2048;
constexpr unsigned WS_FQ = 0x1000, WS_FO = 0x8000, WS_Q = 0x100000, WS_KV = 0x500000, WS_O = 0xD00000, WS_BYTES = 0x1100000;
constexpr unsigned Q_BR = 0x200000, KV_BR = 0x400000, Q_HEAD = 0x10000, KV_HEAD = 0x20000;
constexpr float QSCALE = 0.25f * 1.4426950408889634f;
constexpr int STAGE = 32768, LDS_SPARE = 131072, LDS_BYTES = 139264;
constexpr unsigned SPIN_CAP = 1u << 20;
#define RSTAMP(slot) do {} while (0)

DI unsigned cvtpk(float lo, float hi) { f32x2 v = {lo, hi}; b16x2 b = __builtin_convertvector(v, b16x2); return __builtin_bit_cast(unsigned, b); }
template <int SC1> DI void glds16(const void* sbase, unsigned voff, unsigned lds_dst) {
    unsigned keep;
    if (SC1) asm volatile("s_mov_b32 %0, m0\n\ts_mov_b32 m0, %3\n\ts_nop 0\n\tglobal_load_lds_dwordx4 %1, %2 sc1\n\ts_mov_b32 m0, %0" : "=&s"(keep) : "v"(voff), "s"(sbase), "s"(lds_dst) : "memory");
    else asm volatile("s_mov_b32 %0, m0\n\ts_mov_b32 m0, %3\n\ts_nop 0\n\tglobal_load_lds_dwordx4 %1, %2\n\ts_mov_b32 m0, %0" : "=&s"(keep) : "v"(voff), "s"(sbase), "s"(lds_dst) : "memory");
}
DI void touch(const void* sbase, unsigned voff, unsigned lds_dst) {
    unsigned keep;
    asm volatile("s_mov_b32 %0, m0\n\ts_mov_b32 m0, %3\n\ts_nop 0\n\tglobal_load_lds_dword %1, %2\n\ts_mov_b32 m0, %0" : "=&s"(keep) : "v"(voff), "s"(sbase), "s"(lds_dst) : "memory");
}
DI bf16x8 lds_b128(lds_cptr p) { return *(const LDSP(bf16x8))p; }
DI s16x4 lds_tr(lds_cptr p) { return __builtin_amdgcn_ds_read_tr16_b64_v4i16((LDSP(s16x4))p); }
DI bf16x8 cat8(s16x4 a, s16x4 b) { return (bf16x8){a[0], a[1], a[2], a[3], b[0], b[1], b[2], b[3]}; }
DI u32x4 pack8(const f32x16& a, int o, float sc) {
    return (u32x4){cvtpk(a[o] * sc, a[o + 1] * sc), cvtpk(a[o + 2] * sc, a[o + 3] * sc), cvtpk(a[o + 4] * sc, a[o + 5] * sc), cvtpk(a[o + 6] * sc, a[o + 7] * sc)};
}
typedef __amdgpu_buffer_rsrc_t rsrc_t;
DI bf16x8 ld_sc1(rsrc_t r, unsigned off) { return __builtin_bit_cast(bf16x8, __builtin_amdgcn_raw_buffer_load_b128(r, off, 0, 16)); }
DI void st_sc1(rsrc_t r, unsigned off, u32x4 v) { __builtin_amdgcn_raw_buffer_store_b128(v, r, off, 0, 16); }
DI u64 flag_ld(const char* ws, unsigned off) { return __hip_atomic_load((GASP(u64))(ws + off), __ATOMIC_RELAXED, __HIP_MEMORY_SCOPE_AGENT); }
DI void flag_st(char* ws, unsigned off, u64 v) { __hip_atomic_store((GASP(u64))(ws + off), v, __ATOMIC_RELAXED, __HIP_MEMORY_SCOPE_AGENT); }
DI void wait_flags(const char* ws, unsigned off, bool active, u64 tag) {
#pragma unroll 1
    for (unsigned it = 0; it < SPIN_CAP; ++it) {
        const u64 v = active ? flag_ld(ws, off) : tag;
        if (__all(v == tag)) break;
        __builtin_amdgcn_s_sleep(1);
    }
    asm volatile("" ::: "memory");
}
DI float rowmax16(const f32x16& s) {
    float a = fmaxf(fmaxf(s[0], s[1]), s[2]), b = fmaxf(fmaxf(s[3], s[4]), s[5]);
    a = fmaxf(fmaxf(a, s[6]), s[7]); b = fmaxf(fmaxf(b, s[8]), s[9]); a = fmaxf(fmaxf(a, s[10]), s[11]); b = fmaxf(fmaxf(b, s[12]), s[13]); a = fmaxf(fmaxf(a, s[14]), s[15]);
    const float m = fmaxf(a, b); auto rr = __builtin_amdgcn_permlane32_swap(__float_as_uint(m), __float_as_uint(m), false, false);
    return fmaxf(__uint_as_float(rr[0]), __uint_as_float(rr[1]));
}
DI f32x16 splat16(float v) { f32x16 r;
#pragma unroll
    for (int i = 0; i < 16; ++i) r[i] = v; return r; }
DI void exp_pack(f32x16& s, bf16x8& p0, bf16x8& p1) {
#pragma unroll
    for (int i = 0; i < 16; ++i) s[i] = __builtin_amdgcn_exp2f(s[i]);
    p0 = __builtin_bit_cast(bf16x8, pack8(s, 0, 1.f)); p1 = __builtin_bit_cast(bf16x8, pack8(s, 8, 1.f));
}
DI bf16x8 vfix(bf16x8 v, bool lo) { const bf16x8 ones = {0x3F80, 0x3F80, 0x3F80, 0x3F80, 0x3F80, 0x3F80, 0x3F80, 0x3F80}; return lo ? v : ones; }

__global__ __launch_bounds__(512, 2) void fused_kernel(const uint16_t* xA, const uint16_t* wA, const uint16_t* woA, const uint16_t* xB, const uint16_t* wB, const uint16_t* woB, char* ws, uint16_t* out) {
    extern __shared__ __attribute__((aligned(16))) char smem[];
    const int bid = blockIdx.x, xc = bid & 7, j = bid >> 3;
    const int lane = threadIdx.x & 63, wv = __builtin_amdgcn_readfirstlane(threadIdx.x >> 6), hi = lane >> 5, l31 = lane & 31;
    const unsigned lds0 = (unsigned)(uintptr_t)(LDSP(char))smem;
    const u64 tag = *(const u64*)ws + 1;
    const rsrc_t rs = __builtin_amdgcn_make_buffer_rsrc(ws, 0, WS_BYTES, 0x00020000);
    RSTAMP(0);
    const int rb = (xc >> 1) * 8 + (j & 7), hg = (xc & 1) * 4 + (j >> 3), c0 = ((j & 7) + 2 * (j >> 3)) & 7;
    const int h = xc * 4 + (j & 3), qb = j >> 2;
    if (wv >= 4) {
        const int lw = wv - 4;
        unsigned voffx[2], voffw[3];
#pragma unroll
        for (int m = 0; m < 2; ++m) voffx[m] = (8 * (2 * lw + m) + (lane >> 3)) * 1024 + (((lane & 7) ^ ((4 * m + (lane >> 4)) & 7)) << 4);
#pragma unroll
        for (int t = 0; t < 3; ++t) { const int off = t * 1024 + lane * 16, r8 = off / 384, c = (off % 384) >> 4; voffw[t] = r8 * 3072 + (c >> 3) * 1024 + hg * 128 + (((c & 7) ^ (((r8 >> 1) & 1) << 2)) << 4); }
        auto stage = [&](int g) {
            const int p = g >> 3, kc = (c0 + g) & 7;
            const char* xb = (const char*)(p ? xB : xA) + (size_t)rb * 64 * 1024 + kc * 128;
            const char* wb = (const char*)(p ? wB : wA) + (size_t)(kc * 64 + 16 * lw) * 3072;
            const unsigned d = lds0 + (g & 3) * STAGE;
#pragma unroll
            for (int m = 0; m < 2; ++m) glds16<0>(xb, voffx[m], d + (2 * lw + m) * 1024);
#pragma unroll
            for (int t = 0; t < 6; ++t) glds16<0>(wb + (t / 3) * 8 * 3072, voffw[t % 3], d + 8192 + (6 * lw + t) * 1024);
        };
        auto kvstage = [&](int s) {
            const char* src = ws + WS_KV + (size_t)h * KV_HEAD + s * STAGE + lw * 8192;
#pragma unroll
            for (int i = 0; i < 8; ++i) glds16<1>(src + i * 1024, lane * 16, lds0 + s * STAGE + lw * 8192 + i * 1024);
        };
        stage(0); stage(1);
        WAIT_VM(8); BARRIER();
        stage(2); stage(3);
        WAIT_VM(16); BARRIER();
#pragma unroll 1
        for (int g = 2; g < 14; ++g) {
            stage(g + 2);
            WAIT_VM(16);
            RSTAMP(2 + g);
            BARRIER();
        }
        WAIT_VM(8); BARRIER();
        WAIT_VM(0); RSTAMP(17); BARRIER();
        BARRIER();
        wait_flags(ws, WS_FQ + (((h >> 1) * 64) + lane) * 8, true, tag);
        kvstage(0); kvstage(1); kvstage(2); kvstage(3);
        WAIT_VM(0); RSTAMP(18);
        BARRIER();
        {
            const int qw = qb * 256 + 64 * lw, T0 = qw / 32 - 8; const bool vlo = l31 < 16;
            { const int t = T0 + lane; wait_flags(ws, WS_FQ + ((16 + (h >> 1)) * 64 + t) * 8, lane < 10 && t >= 0, tag); }
            RSTAMP(19);
            const unsigned kvb = WS_KV + KV_BR + h * KV_HEAD, kofs = kvb + lane * 16, vofs = kvb + 1024 + hi * 256 + (l31 & 15) * 16;
            bf16x8 kf[10];
#pragma unroll
            for (int t = 0; t < 10; ++t) { const int g = T0 + t < 0 ? 0 : T0 + t; kf[t] = ld_sc1(rs, kofs + g * 2048); }
            const unsigned qofs = WS_Q + Q_BR + h * Q_HEAD + (qw / 32) * 1024 + lane * 16;
            const bf16x8 q0f = ld_sc1(rs, qofs), q1f = ld_sc1(rs, qofs + 1024);
            f32x16 mF, mL;
#pragma unroll
            for (int r = 0; r < 16; ++r) { const int jj = (r & 3) + 8 * (r >> 2) + 4 * hi; mF[r] = jj > l31 ? 0.f : -INFINITY; mL[r] = jj <= l31 ? 0.f : -INFINITY; }
#pragma unroll 1
            for (int blk = 0; blk < 2; ++blk) {
                const bf16x8 qf = blk ? q1f : q0f;
                if (blk) {
#pragma unroll
                    for (int t = 0; t < 9; ++t) kf[t] = kf[t + 1]; }
                const int tmin = (T0 + blk < 0) ? -(T0 + blk) : 0;
                bf16x8 v0[9], v1[9];
#pragma unroll
                for (int t = 0; t < 9; ++t) { const int g = T0 + blk + t < 0 ? 0 : T0 + blk + t; v0[t] = ld_sc1(rs, vofs + g * 2048); v1[t] = ld_sc1(rs, vofs + g * 2048 + 512); }
                float m = rowmax16(MFMA(kf[8], qf, mL));
#pragma unroll
                for (int t = 1; t < 8; ++t) if (t >= tmin) m = fmaxf(m, rowmax16(MFMA(kf[t], qf, splat16(0.f))));
                if (tmin == 0) m = fmaxf(m, rowmax16(MFMA(kf[0], qf, mF)));
                f32x16 o = {}; const f32x16 cm = splat16(-m);
#pragma unroll
                for (int t = 0; t <= 8; ++t) if (t >= tmin) {
                    f32x16 c = cm;
                    if (t == 0) {
#pragma unroll
                        for (int r = 0; r < 16; ++r) c[r] += mF[r]; }
                    if (t == 8) {
#pragma unroll
                        for (int r = 0; r < 16; ++r) c[r] += mL[r]; }
                    f32x16 s = MFMA(kf[t], qf, c); bf16x8 p0, p1; exp_pack(s, p0, p1);
                    o = MFMA(vfix(v0[t], vlo), p0, o); o = MFMA(vfix(v1[t], vlo), p1, o);
                }
                st_sc1(rs, WS_O + Q_BR + h * Q_HEAD + (qw / 32 + blk) * 1024 + lane * 16, pack8(o, 0, 1.f / o[8]));
            }
            WAIT_VM(0);
            if (lane == 0) flag_st(ws, WS_FO + ((32 + qw / 64) * 32 + h) * 8, tag);
            RSTAMP(20);
        }
    } else {
        const int tt = wv & 1, hp = wv >> 1, q = (lane >> 2) & 3;
        const int xrow = 32 * tt + l31; const unsigned xaddr0 = xrow * 128 + ((hi ^ ((l31 >> 1) & 7)) << 4);
        const unsigned wbase = 8192 + (8 * hi + q) * 384 + ((hp ^ (q >> 1)) * 64) + ((lane >> 4) & 1) * 32 + (lane & 3) * 8;
        f32x16 acc0, acc1, acc2, fx, fw0, fw1, fw2;
        unsigned xb0 = lds0 + xaddr0, xb1 = lds0 + (xaddr0 ^ 32), xb2 = lds0 + (xaddr0 ^ 64), xb3 = lds0 + (xaddr0 ^ 96), wb_ = lds0 + wbase;
        unsigned soff = 0;
        asm volatile(QKV_FIRST_ASM : "={v[48:63]}"(fx), "={v[64:79]}"(fw0), "={v[80:95]}"(fw1), "={v[96:111]}"(fw2)
                     : [soff] "s"(soff), "{v176}"(xb0), "{v177}"(xb1), "{v178}"(xb2), "{v179}"(xb3), "{v180}"(wb_) : "v181", "v182", "v183", "v184", "v185", "memory");
        RSTAMP(1);
        {
            const unsigned sp = lds0 + LDS_SPARE + wv * 1024;
            if (wv == 0) { const char* xb = (const char*)xB + (size_t)(xc >> 1) * 512 * 1024 + j * 16384; touch(xb, lane * 128, sp); touch(xb + 8192, lane * 128, sp + 256); }
            else if (wv == 1) { const char* wb = (const char*)wB + (xc & 1) * 512;
#pragma unroll
                   for (int i = 0; i < 3; ++i) { const int li = j * 192 + i * 64 + lane, k = li / 12, r = li % 12; touch(wb, k * 3072 + (r >> 2) * 1024 + (r & 3) * 128, sp + i * 256); } }
            else if (lane < 16) touch((const char*)(wv == 2 ? woA : woB) + bid * 2048, lane * 128, sp);
        }
#define QKV_RUN(N) do { int cnt = (N); asm volatile(QKV_PHASE_ASM : "+{v[0:15]}"(acc0), "+{v[16:31]}"(acc1), "+{v[32:47]}"(acc2), "+{v[48:63]}"(fx), "+{v[64:79]}"(fw0), "+{v[80:95]}"(fw1), "+{v[96:111]}"(fw2), [soff] "+s"(soff), [cnt] "+s"(cnt) \
                         : "{v176}"(xb0), "{v177}"(xb1), "{v178}"(xb2), "{v179}"(xb3), "{v180}"(wb_) : QKV_CLOB_B, "scc", "memory"); } while (0)
        const int tile = rb * 2 + tt, h0 = 4 * hg + 2 * hp;
        auto epilogue = [&](int br) {
            const unsigned qo = WS_Q + br * Q_BR + h0 * Q_HEAD + tile * 1024 + lane * 16, ko = WS_KV + br * KV_BR + h0 * KV_HEAD + tile * 2048 + lane * 16;
            st_sc1(rs, ko, pack8(acc1, 0, 1.f)); st_sc1(rs, ko + KV_HEAD, pack8(acc1, 8, 1.f));
            const unsigned vo = WS_KV + br * KV_BR + (h0 + (l31 >> 4)) * KV_HEAD + tile * 2048 + 1024 + hi * 256 + (l31 & 15) * 16;
            st_sc1(rs, vo, pack8(acc2, 0, 1.f)); st_sc1(rs, vo + 512, pack8(acc2, 8, 1.f));
            st_sc1(rs, qo, pack8(acc0, 0, QSCALE)); st_sc1(rs, qo + Q_HEAD, pack8(acc0, 8, QSCALE));
        };
        acc0 = f32x16{}; acc1 = f32x16{}; acc2 = f32x16{};
        QKV_RUN(4);
        RSTAMP(2);
        epilogue(0);
        acc0 = f32x16{}; acc1 = f32x16{}; acc2 = f32x16{};
        QKV_RUN(1);
        WAIT_VM(0);
        if (lane == 0) flag_st(ws, WS_FQ + (((h0 >> 1) * 64) + tile) * 8, tag);
        RSTAMP(3);
        QKV_RUN(3);
        RSTAMP(4);
        epilogue(1);
        BARRIER();
        RSTAMP(5);
        {
            const int qw = qb * 256 + 64 * wv;
            const unsigned qofs = WS_Q + h * Q_HEAD + (qw / 32) * 1024 + lane * 16;
            bf16x8 qa = ld_sc1(rs, qofs), qbf = ld_sc1(rs, qofs + 1024);
            __builtin_amdgcn_s_setprio(2);
            const bf16x8 k0 = lds_b128((lds_cptr)smem + lane * 16);
            f32x16 ca = splat16(-rowmax16(MFMA(k0, qa, splat16(0.f)))), cb = splat16(-rowmax16(MFMA(k0, qbf, splat16(0.f))));
            unsigned kaddr = lds0 + lane * 16, vaddr = lds0 + 1024 + hi * 256 + (l31 & 15) * 16;
            unsigned fill = (l31 == 16 || l31 == 20) ? 0x3f803f80u : 0u;
            unsigned foff = WS_FQ + ((16 + (h0 >> 1)) * 64 + tile) * 8;
            f32x16 oa = {}, ob = {};
            int cnt = 16; const u64 vmask = 0x0000FFFF0000FFFFull;
            RSTAMP(6);
            asm volatile(ATTN_A_ASM
                : "+{v[0:15]}"(oa), "+{v[16:31]}"(ob), [cnt] "+s"(cnt), "+{v136}"(kaddr), "+{v137}"(vaddr)
                : "{v[32:47]}"(ca), "{v[48:63]}"(cb), "{v[112:115]}"(qa), "{v[116:119]}"(qbf), [vmask] "s"(vmask), "{v138}"(fill), "{v139}"(foff), "{v[140:141]}"(tag), [fbase] "s"(ws)
                : ATTN_A_CLOB, "scc", "memory");
            RSTAMP(7);
            const unsigned oo = WS_O + h * Q_HEAD + (qw / 32) * 1024 + lane * 16;
            st_sc1(rs, oo, pack8(oa, 0, 1.f / oa[8])); st_sc1(rs, oo + 1024, pack8(ob, 0, 1.f / ob[8]));
            WAIT_VM(0);
            if (lane == 0) flag_st(ws, WS_FO + ((qw / 64) * 32 + h) * 8, tag);
            __builtin_amdgcn_s_setprio(0);
            RSTAMP(8);
        }
    }
    BARRIER();
    {
        const int br = xc >> 2, orb = (xc & 3) * 4 + (j & 3), cb = j >> 2, tt = wv & 3, kh = wv >> 2;
        const char* wo = (const char*)(br ? woB : woA) + cb * 128;
        const unsigned voff = (lane >> 3) * 1024 + (((lane & 7) ^ (((lane >> 4) & 1) << 2)) << 4);
#pragma unroll
        for (int i = 0; i < 8; ++i) glds16<0>(wo + (wv * 8 + i) * 8192, voff, lds0 + (wv * 8 + i) * 1024);
        const int tile = orb * 4 + tt;
        wait_flags(ws, WS_FO + ((br * 32 + (tile >> 1)) * 32 + kh * 16 + (lane & 15)) * 8, lane < 16, tag);
        RSTAMP(21);
        bf16x8 of[16];
        const unsigned oofs = WS_O + br * Q_BR + kh * 16 * Q_HEAD + tile * 1024 + lane * 16;
#pragma unroll
        for (int i = 0; i < 16; ++i) of[i] = ld_sc1(rs, oofs + i * Q_HEAD);
        const int q = (lane >> 2) & 3;
        unsigned wb[2];
#pragma unroll
        for (int ct = 0; ct < 2; ++ct) wb[ct] = (kh * 256 + 4 * hi + q) * 128 + ((ct ^ (q >> 1)) * 64) + ((lane >> 4) & 1) * 32 + (lane & 3) * 8;
        WAIT_VM(0); BARRIER();
        RSTAMP(22);
        f32x16 acc[2] = {};
#pragma unroll
        for (int i = 0; i < 16; ++i)
#pragma unroll
            for (int ct = 0; ct < 2; ++ct) { lds_cptr p = (lds_cptr)smem + wb[ct] + i * 2048; acc[ct] = MFMA(cat8(lds_tr(p), lds_tr(p + 1024)), of[i], acc[ct]); }
        LDSP(float) xs = (LDSP(float))(smem + 65536) + wv * 1024; LDSP(float) xr = (LDSP(float))(smem + 65536) + (wv ^ 4) * 1024;
#pragma unroll
        for (int r = 0; r < 16; ++r) xs[r * 64 + lane] = kh ? acc[0][r] : acc[1][r];
        BARRIER();
        f32x16 o = kh ? acc[1] : acc[0];
#pragma unroll
        for (int r = 0; r < 16; ++r) o[r] += xr[r * 64 + lane];
        uint16_t* orow = out + (size_t)(tile * 32 + l31) * 1024 + br * 512 + cb * 64 + kh * 32 + 4 * hi;
#pragma unroll
        for (int g = 0; g < 4; ++g) *(u32x2*)(orow + 8 * g) = (u32x2){cvtpk(o[4 * g], o[4 * g + 1]), cvtpk(o[4 * g + 2], o[4 * g + 3])};
        RSTAMP(23);
    }
    if (bid == 0 && wv == 7) {
        _Pragma("unroll 1") for (int i = 0; i < 16; ++i) wait_flags(ws, WS_FQ + (i * 64 + lane) * 8, true, tag);
        if (lane == 0) flag_st(ws, 0, tag);
    }
}

extern "C" void kernel_launch(void* const* d_in, const int* in_sizes, int n_in, void* d_out, int out_size, void* d_ws, size_t ws_size, hipStream_t stream) {
    (void)hipFuncSetAttribute((const void*)fused_kernel, hipFuncAttributeMaxDynamicSharedMemorySize, LDS_BYTES);
    fused_kernel<<<256, 512, LDS_BYTES, stream>>>((const uint16_t*)d_in[0], (const uint16_t*)d_in[1], (const uint16_t*)d_in[2], (const uint16_t*)d_in[3], (const uint16_t*)d_in[4], (const uint16_t*)d_in[5], (char*)d_ws, (uint16_t*)d_out);
}
```

```cpp
#include <hip/hip_runtime.h>
#include <stdint.h>
#define QKV_FIRST_ASM \
    "s_waitcnt lgkmcnt(0)\n\t" \
    "s_barrier\n\t" \
    "v_add_u32 v185, %[soff], v180\n\t" \
    "v_add_u32 v181, %[soff], v176\n\t" \
    "v_add_u32 v182, %[soff], v177\n\t" \
    "v_add_u32 v183, %[soff], v178\n\t" \
    "v_add_u32 v184, %[soff], v179\n\t" \
    "ds_read_b128 v[48:51], v181\n\t" \
    "ds_read_b64_tr_b16 v[64:65], v185 offset:0\n\t" \
    "ds_read_b64_tr_b16 v[66:67], v185 offset:1536\n\t" \
    "ds_read_b64_tr_b16 v[68:69], v185 offset:128\n\t" \
    "ds_read_b64_tr_b16 v[70:71], v185 offset:1664\n\t" \
    "ds_read_b64_tr_b16 v[72:73], v185 offset:256\n\t" \
    "ds_read_b64_tr_b16 v[74:75], v185 offset:1792\n\t" \
    "ds_read_b128 v[52:55], v182\n\t" \
    "ds_read_b64_tr_b16 v[76:77], v185 offset:6144\n\t" \
    "ds_read_b64_tr_b16 v[78:79], v185 offset:7680\n\t" \
    "ds_read_b64_tr_b16 v[80:81], v185 offset:6272\n\t" \
    "ds_read_b64_tr_b16 v[82:83], v185 offset:7808\n\t" \
    "ds_read_b64_tr_b16 v[84:85], v185 offset:6400\n\t" \
    "ds_read_b64_tr_b16 v[86:87], v185 offset:7936\n\t" \
    "ds_read_b128 v[56:59], v183\n\t" \
    "ds_read_b64_tr_b16 v[88:89], v185 offset:12288\n\t" \
    "ds_read_b64_tr_b16 v[90:91], v185 offset:13824\n\t" \
    "ds_read_b64_tr_b16 v[92:93], v185 offset:12416\n\t" \
    "ds_read_b64_tr_b16 v[94:95], v185 offset:13952\n\t" \
    "ds_read_b64_tr_b16 v[96:97], v185 offset:12544\n\t" \
    "ds_read_b64_tr_b16 v[98:99], v185 offset:14080\n\t" \
    "ds_read_b128 v[60:63], v184\n\t" \
    "ds_read_b64_tr_b16 v[100:101], v185 offset:18432\n\t" \
    "ds_read_b64_tr_b16 v[102:103], v185 offset:19968\n\t" \
    "ds_read_b64_tr_b16 v[104:105], v185 offset:18560\n\t" \
    "ds_read_b64_tr_b16 v[106:107], v185 offset:20096\n\t" \
    "ds_read_b64_tr_b16 v[108:109], v185 offset:18688\n\t" \
    "ds_read_b64_tr_b16 v[110:111], v185 offset:20224\n\t" \
    "s_waitcnt lgkmcnt(0)\n\t"

#define QKV_PHASE_ASM \
    "1:\n\t" \
    "s_waitcnt lgkmcnt(0)\n\t" \
    "s_barrier\n\t" \
    "v_mfma_f32_32x32x16_bf16 v[0:15], v[64:67], v[48:51], v[0:15]\n\t" \
    "s_add_u32 %[soff], %[soff], 0x8000\n\t" \
    "s_and_b32 %[soff], %[soff], 0x18000\n\t" \
    "v_add_u32 v185, %[soff], v180\n\t" \
    "v_add_u32 v181, %[soff], v176\n\t" \
    "v_add_u32 v182, %[soff], v177\n\t" \
    "v_add_u32 v183, %[soff], v178\n\t" \
    "v_add_u32 v184, %[soff], v179\n\t" \
    "ds_read_b128 v[112:115], v181\n\t" \
    "ds_read_b64_tr_b16 v[128:129], v185 offset:0\n\t" \
    "ds_read_b64_tr_b16 v[130:131], v185 offset:1536\n\t" \
    "v_mfma_f32_32x32x16_bf16 v[16:31], v[68:71], v[48:51], v[16:31]\n\t" \
    "ds_read_b64_tr_b16 v[132:133], v185 offset:128\n\t" \
    "ds_read_b64_tr_b16 v[134:135], v185 offset:1664\n\t" \
    "ds_read_b64_tr_b16 v[136:137], v185 offset:256\n\t" \
    "v_mfma_f32_32x32x16_bf16 v[32:47], v[48:51], v[72:75], v[32:47]\n\t" \
    "ds_read_b64_tr_b16 v[138:139], v185 offset:1792\n\t" \
    "ds_read_b128 v[116:119], v182\n\t" \
    "ds_read_b64_tr_b16 v[140:141], v185 offset:6144\n\t" \
    "v_mfma_f32_32x32x16_bf16 v[0:15], v[76:79], v[52:55], v[0:15]\n\t" \
    "ds_read_b64_tr_b16 v[142:143], v185 offset:7680\n\t" \
    "ds_read_b64_tr_b16 v[144:145], v185 offset:6272\n\t" \
    "ds_read_b64_tr_b16 v[146:147], v185 offset:7808\n\t" \
    "v_mfma_f32_32x32x16_bf16 v[16:31], v[80:83], v[52:55], v[16:31]\n\t" \
    "ds_read_b64_tr_b16 v[148:149], v185 offset:6400\n\t" \
    "ds_read_b64_tr_b16 v[150:151], v185 offset:7936\n\t" \
    "ds_read_b128 v[120:123], v183\n\t" \
    "v_mfma_f32_32x32x16_bf16 v[32:47], v[52:55], v[84:87], v[32:47]\n\t" \
    "ds_read_b64_tr_b16 v[152:153], v185 offset:12288\n\t" \
    "ds_read_b64_tr_b16 v[154:155], v185 offset:13824\n\t" \
    "ds_read_b64_tr_b16 v[156:157], v185 offset:12416\n\t" \
    "v_mfma_f32_32x32x16_bf16 v[0:15], v[88:91], v[56:59], v[0:15]\n\t" \
    "ds_read_b64_tr_b16 v[158:159], v185 offset:13952\n\t" \
    "ds_read_b64_tr_b16 v[160:161], v185 offset:12544\n\t" \
    "ds_read_b64_tr_b16 v[162:163], v185 offset:14080\n\t" \
    "v_mfma_f32_32x32x16_bf16 v[16:31], v[92:95], v[56:59], v[16:31]\n\t" \
    "ds_read_b128 v[124:127], v184\n\t" \
    "ds_read_b64_tr_b16 v[164:165], v185 offset:18432\n\t" \
    "v_mfma_f32_32x32x16_bf16 v[32:47], v[56:59], v[96:99], v[32:47]\n\t" \
    "ds_read_b64_tr_b16 v[166:167], v185 offset:19968\n\t" \
    "ds_read_b64_tr_b16 v[168:169], v185 offset:18560\n\t" \
    "v_mfma_f32_32x32x16_bf16 v[0:15], v[100:103], v[60:63], v[0:15]\n\t" \
    "ds_read_b64_tr_b16 v[170:171], v185 offset:20096\n\t" \
    "ds_read_b64_tr_b16 v[172:173], v185 offset:18688\n\t" \
    "v_mfma_f32_32x32x16_bf16 v[16:31], v[104:107], v[60:63], v[16:31]\n\t" \
    "ds_read_b64_tr_b16 v[174:175], v185 offset:20224\n\t" \
    "v_mfma_f32_32x32x16_bf16 v[32:47], v[60:63], v[108:111], v[32:47]\n\t" \
    "s_waitcnt lgkmcnt(0)\n\t" \
    "s_barrier\n\t" \
    "v_mfma_f32_32x32x16_bf16 v[0:15], v[128:131], v[112:115], v[0:15]\n\t" \
    "s_add_u32 %[soff], %[soff], 0x8000\n\t" \
    "s_and_b32 %[soff], %[soff], 0x18000\n\t" \
    "v_add_u32 v185, %[soff], v180\n\t" \
    "v_add_u32 v181, %[soff], v176\n\t" \
    "v_add_u32 v182, %[soff], v177\n\t" \
    "v_add_u32 v183, %[soff], v178\n\t" \
    "v_add_u32 v184, %[soff], v179\n\t" \
    "ds_read_b128 v[48:51], v181\n\t" \
    "ds_read_b64_tr_b16 v[64:65], v185 offset:0\n\t" \
    "ds_read_b64_tr_b16 v[66:67], v185 offset:1536\n\t" \
    "v_mfma_f32_32x32x16_bf16 v[16:31], v[132:135], v[112:115], v[16:31]\n\t" \
    "ds_read_b64_tr_b16 v[68:69], v185 offset:128\n\t" \
    "ds_read_b64_tr_b16 v[70:71], v185 offset:1664\n\t" \
    "ds_read_b64_tr_b16 v[72:73], v185 offset:256\n\t" \
    "v_mfma_f32_32x32x16_bf16 v[32:47], v[112:115], v[136:139], v[32:47]\n\t" \
    "ds_read_b64_tr_b16 v[74:75], v185 offset:1792\n\t" \
    "ds_read_b128 v[52:55], v182\n\t" \
    "ds_read_b64_tr_b16 v[76:77], v185 offset:6144\n\t" \
    "v_mfma_f32_32x32x16_bf16 v[0:15], v[140:143], v[116:119], v[0:15]\n\t" \
    "ds_read_b64_tr_b16 v[78:79], v185 offset:7680\n\t" \
    "ds_read_b64_tr_b16 v[80:81], v185 offset:6272\n\t" \
    "ds_read_b64_tr_b16 v[82:83], v185 offset:7808\n\t" \
    "v_mfma_f32_32x32x16_bf16 v[16:31], v[144:147], v[116:119], v[16:31]\n\t" \
    "ds_read_b64_tr_b16 v[84:85], v185 offset:6400\n\t" \
    "ds_read_b64_tr_b16 v[86:87], v185 offset:7936\n\t" \
    "ds_read_b128 v[56:59], v183\n\t" \
    "v_mfma_f32_32x32x16_bf16 v[32:47], v[116:119], v[148:151], v[32:47]\n\t" \
    "ds_read_b64_tr_b16 v[88:89], v185 offset:12288\n\t" \
    "ds_read_b64_tr_b16 v[90:91], v185 offset:13824\n\t" \
    "ds_read_b64_tr_b16 v[92:93], v185 offset:12416\n\t" \
    "v_mfma_f32_32x32x16_bf16 v[0:15], v[152:155], v[120:123], v[0:15]\n\t" \
    "ds_read_b64_tr_b16 v[94:95], v185 offset:13952\n\t" \
    "ds_read_b64_tr_b16 v[96:97], v185 offset:12544\n\t" \
    "ds_read_b64_tr_b16 v[98:99], v185 offset:14080\n\t" \
    "v_mfma_f32_32x32x16_bf16 v[16:31], v[156:159], v[120:123], v[16:31]\n\t" \
    "ds_read_b128 v[60:63], v184\n\t" \
    "ds_read_b64_tr_b16 v[100:101], v185 offset:18432\n\t" \
    "v_mfma_f32_32x32x16_bf16 v[32:47], v[120:123], v[160:163], v[32:47]\n\t" \
    "ds_read_b64_tr_b16 v[102:103], v185 offset:19968\n\t" \
    "ds_read_b64_tr_b16 v[104:105], v185 offset:18560\n\t" \
    "v_mfma_f32_32x32x16_bf16 v[0:15], v[164:167], v[124:127], v[0:15]\n\t" \
    "ds_read_b64_tr_b16 v[106:107], v185 offset:20096\n\t" \
    "ds_read_b64_tr_b16 v[108:109], v185 offset:18688\n\t" \
    "v_mfma_f32_32x32x16_bf16 v[16:31], v[168:171], v[124:127], v[16:31]\n\t" \
    "ds_read_b64_tr_b16 v[110:111], v185 offset:20224\n\t" \
    "v_mfma_f32_32x32x16_bf16 v[32:47], v[124:127], v[172:175], v[32:47]\n\t" \
    "s_sub_u32 %[cnt], %[cnt], 1\n\t" \
    "s_cmp_lg_u32 %[cnt], 0\n\t" \
    "s_cbranch_scc1 1b\n\t" \
    "s_waitcnt lgkmcnt(0)\n\t" \
    "s_nop 15\n\t"

#define QKV_CLOB_B "v112","v113","v114","v115","v116","v117","v118","v119","v120","v121","v122","v123","v124","v125","v126","v127","v128","v129","v130","v131","v132","v133","v134","v135","v136","v137","v138","v139","v140","v141","v142","v143","v144","v145","v146","v147","v148","v149","v150","v151","v152","v153","v154","v155","v156","v157","v158","v159","v160","v161","v162","v163","v164","v165","v166","v167","v168","v169","v170","v171","v172","v173","v174","v175","v181","v182","v183","v184","v185"

#define ATTN_A_ASM \
    "v_mov_b32 v124, v138\n\t" \
    "v_mov_b32 v125, v138\n\t" \
    "v_mov_b32 v126, v138\n\t" \
    "v_mov_b32 v127, v138\n\t" \
    "v_mov_b32 v128, v138\n\t" \
    "v_mov_b32 v129, v138\n\t" \
    "v_mov_b32 v130, v138\n\t" \
    "v_mov_b32 v131, v138\n\t" \
    "v_mov_b32 v132, v138\n\t" \
    "v_mov_b32 v133, v138\n\t" \
    "v_mov_b32 v134, v138\n\t" \
    "v_mov_b32 v135, v138\n\t" \
    "v_mov_b32 v108, 0\n\t" \
    "v_mov_b32 v109, 0\n\t" \
    "v_mov_b32 v110, 0\n\t" \
    "v_mov_b32 v111, 0\n\t" \
    "ds_read_b128 v[120:123], v136\n\t" \
    "s_mov_b64 exec, %[vmask]\n\t" \
    "ds_read_b128 v[124:127], v137\n\t" \
    "ds_read_b128 v[128:131], v137 offset:512\n\t" \
    "s_mov_b64 exec, -1\n\t" \
    "s_waitcnt lgkmcnt(2)\n\t" \
    "v_mfma_f32_32x32x16_bf16 v[64:79], v[120:123], v[112:115], v[32:47]\n\t" \
    "s_nop 15\n\t" \
    "1:\n\t" \
    "v_exp_f32 v64, v64\n\t" \
    "v_exp_f32 v65, v65\n\t" \
    "v_exp_f32 v66, v66\n\t" \
    "v_cvt_pk_bf16_f32 v96, v64, v65\n\t" \
    "v_exp_f32 v67, v67\n\t" \
    "v_exp_f32 v68, v68\n\t" \
    "v_cvt_pk_bf16_f32 v97, v66, v67\n\t" \
    "v_exp_f32 v69, v69\n\t" \
    "v_mfma_f32_32x32x16_bf16 v[80:95], v[120:123], v[116:119], v[48:63]\n\t" \
    "ds_read_b128 v[120:123], v136 offset:2048\n\t" \
    "v_exp_f32 v70, v70\n\t" \
    "v_cvt_pk_bf16_f32 v98, v68, v69\n\t" \
    "v_exp_f32 v71, v71\n\t" \
    "v_exp_f32 v72, v72\n\t" \
    "v_cvt_pk_bf16_f32 v99, v70, v71\n\t" \
    "v_exp_f32 v73, v73\n\t" \
    "v_exp_f32 v74, v74\n\t" \
    "v_cvt_pk_bf16_f32 v100, v72, v73\n\t" \
    "s_waitcnt lgkmcnt(2)\n\t" \
    "v_mfma_f32_32x32x16_bf16 v[0:15], v[124:127], v[96:99], v[0:15]\n\t" \
    "v_exp_f32 v75, v75\n\t" \
    "v_exp_f32 v76, v76\n\t" \
    "v_cvt_pk_bf16_f32 v101, v74, v75\n\t" \
    "v_exp_f32 v77, v77\n\t" \
    "v_exp_f32 v78, v78\n\t" \
    "v_exp_f32 v79, v79\n\t" \
    "v_cvt_pk_bf16_f32 v102, v76, v77\n\t" \
    "v_cvt_pk_bf16_f32 v103, v78, v79\n\t" \
    "v_mfma_f32_32x32x16_bf16 v[16:31], v[132:135], v[108:111], v[16:31]\n\t" \
    "v_exp_f32 v80, v80\n\t" \
    "v_exp_f32 v81, v81\n\t" \
    "v_exp_f32 v82, v82\n\t" \
    "v_cvt_pk_bf16_f32 v104, v80, v81\n\t" \
    "v_exp_f32 v83, v83\n\t" \
    "v_exp_f32 v84, v84\n\t" \
    "v_cvt_pk_bf16_f32 v105, v82, v83\n\t" \
    "v_exp_f32 v85, v85\n\t" \
    "s_waitcnt lgkmcnt(0)\n\t" \
    "v_mfma_f32_32x32x16_bf16 v[64:79], v[120:123], v[112:115], v[32:47]\n\t" \
    "v_exp_f32 v86, v86\n\t" \
    "v_cvt_pk_bf16_f32 v106, v84, v85\n\t" \
    "v_exp_f32 v87, v87\n\t" \
    "v_exp_f32 v88, v88\n\t" \
    "v_cvt_pk_bf16_f32 v107, v86, v87\n\t" \
    "v_exp_f32 v89, v89\n\t" \
    "v_exp_f32 v90, v90\n\t" \
    "v_cvt_pk_bf16_f32 v108, v88, v89\n\t" \
    "v_mfma_f32_32x32x16_bf16 v[16:31], v[124:127], v[104:107], v[16:31]\n\t" \
    "s_mov_b64 exec, %[vmask]\n\t" \
    "ds_read_b128 v[124:127], v137 offset:2048\n\t" \
    "ds_read_b128 v[132:135], v137 offset:2560\n\t" \
    "s_mov_b64 exec, -1\n\t" \
    "v_exp_f32 v91, v91\n\t" \
    "v_exp_f32 v92, v92\n\t" \
    "v_cvt_pk_bf16_f32 v109, v90, v91\n\t" \
    "v_exp_f32 v93, v93\n\t" \
    "v_exp_f32 v94, v94\n\t" \
    "v_exp_f32 v95, v95\n\t" \
    "v_cvt_pk_bf16_f32 v110, v92, v93\n\t" \
    "v_cvt_pk_bf16_f32 v111, v94, v95\n\t" \
    "v_mfma_f32_32x32x16_bf16 v[0:15], v[128:131], v[100:103], v[0:15]\n\t" \
    "v_exp_f32 v64, v64\n\t" \
    "v_exp_f32 v65, v65\n\t" \
    "v_exp_f32 v66, v66\n\t" \
    "v_cvt_pk_bf16_f32 v96, v64, v65\n\t" \
    "v_exp_f32 v67, v67\n\t" \
    "v_exp_f32 v68, v68\n\t" \
    "v_cvt_pk_bf16_f32 v97, v66, v67\n\t" \
    "v_exp_f32 v69, v69\n\t" \
    "v_mfma_f32_32x32x16_bf16 v[80:95], v[120:123], v[116:119], v[48:63]\n\t" \
    "ds_read_b128 v[120:123], v136 offset:4096\n\t" \
    "v_exp_f32 v70, v70\n\t" \
    "v_cvt_pk_bf16_f32 v98, v68, v69\n\t" \
    "v_exp_f32 v71, v71\n\t" \
    "v_exp_f32 v72, v72\n\t" \
    "v_cvt_pk_bf16_f32 v99, v70, v71\n\t" \
    "v_exp_f32 v73, v73\n\t" \
    "v_exp_f32 v74, v74\n\t" \
    "v_cvt_pk_bf16_f32 v100, v72, v73\n\t" \
    "s_waitcnt lgkmcnt(2)\n\t" \
    "v_mfma_f32_32x32x16_bf16 v[0:15], v[124:127], v[96:99], v[0:15]\n\t" \
    "v_exp_f32 v75, v75\n\t" \
    "v_exp_f32 v76, v76\n\t" \
    "v_cvt_pk_bf16_f32 v101, v74, v75\n\t" \
    "v_exp_f32 v77, v77\n\t" \
    "v_exp_f32 v78, v78\n\t" \
    "v_exp_f32 v79, v79\n\t" \
    "v_cvt_pk_bf16_f32 v102, v76, v77\n\t" \
    "v_cvt_pk_bf16_f32 v103, v78, v79\n\t" \
    "v_mfma_f32_32x32x16_bf16 v[16:31], v[128:131], v[108:111], v[16:31]\n\t" \
    "v_exp_f32 v80, v80\n\t" \
    "v_exp_f32 v81, v81\n\t" \
    "v_exp_f32 v82, v82\n\t" \
    "v_cvt_pk_bf16_f32 v104, v80, v81\n\t" \
    "v_exp_f32 v83, v83\n\t" \
    "v_exp_f32 v84, v84\n\t" \
    "v_cvt_pk_bf16_f32 v105, v82, v83\n\t" \
    "v_exp_f32 v85, v85\n\t" \
    "s_waitcnt lgkmcnt(0)\n\t" \
    "v_mfma_f32_32x32x16_bf16 v[64:79], v[120:123], v[112:115], v[32:47]\n\t" \
    "v_exp_f32 v86, v86\n\t" \
    "v_cvt_pk_bf16_f32 v106, v84, v85\n\t" \
    "v_exp_f32 v87, v87\n\t" \
    "v_exp_f32 v88, v88\n\t" \
    "v_cvt_pk_bf16_f32 v107, v86, v87\n\t" \
    "v_exp_f32 v89, v89\n\t" \
    "v_exp_f32 v90, v90\n\t" \
    "v_cvt_pk_bf16_f32 v108, v88, v89\n\t" \
    "v_mfma_f32_32x32x16_bf16 v[16:31], v[124:127], v[104:107], v[16:31]\n\t" \
    "s_mov_b64 exec, %[vmask]\n\t" \
    "ds_read_b128 v[124:127], v137 offset:4096\n\t" \
    "ds_read_b128 v[128:131], v137 offset:4608\n\t" \
    "s_mov_b64 exec, -1\n\t" \
    "v_exp_f32 v91, v91\n\t" \
    "v_exp_f32 v92, v92\n\t" \
    "v_cvt_pk_bf16_f32 v109, v90, v91\n\t" \
    "v_exp_f32 v93, v93\n\t" \
    "v_exp_f32 v94, v94\n\t" \
    "v_exp_f32 v95, v95\n\t" \
    "v_cvt_pk_bf16_f32 v110, v92, v93\n\t" \
    "v_cvt_pk_bf16_f32 v111, v94, v95\n\t" \
    "v_mfma_f32_32x32x16_bf16 v[0:15], v[132:135], v[100:103], v[0:15]\n\t" \
    "v_exp_f32 v64, v64\n\t" \
    "v_exp_f32 v65, v65\n\t" \
    "v_exp_f32 v66, v66\n\t" \
    "v_cvt_pk_bf16_f32 v96, v64, v65\n\t" \
    "v_exp_f32 v67, v67\n\t" \
    "v_exp_f32 v68, v68\n\t" \
    "v_cvt_pk_bf16_f32 v97, v66, v67\n\t" \
    "v_exp_f32 v69, v69\n\t" \
    "v_mfma_f32_32x32x16_bf16 v[80:95], v[120:123], v[116:119], v[48:63]\n\t" \
    "ds_read_b128 v[120:123], v136 offset:6144\n\t" \
    "v_exp_f32 v70, v70\n\t" \
    "v_cvt_pk_bf16_f32 v98, v68, v69\n\t" \
    "v_exp_f32 v71, v71\n\t" \
    "v_exp_f32 v72, v72\n\t" \
    "v_cvt_pk_bf16_f32 v99, v70, v71\n\t" \
    "v_exp_f32 v73, v73\n\t" \
    "v_exp_f32 v74, v74\n\t" \
    "v_cvt_pk_bf16_f32 v100, v72, v73\n\t" \
    "s_waitcnt lgkmcnt(2)\n\t" \
    "v_mfma_f32_32x32x16_bf16 v[0:15], v[124:127], v[96:99], v[0:15]\n\t" \
    "v_exp_f32 v75, v75\n\t" \
    "v_exp_f32 v76, v76\n\t" \
    "v_cvt_pk_bf16_f32 v101, v74, v75\n\t" \
    "v_exp_f32 v77, v77\n\t" \
    "v_exp_f32 v78, v78\n\t" \
    "v_exp_f32 v79, v79\n\t" \
    "v_cvt_pk_bf16_f32 v102, v76, v77\n\t" \
    "v_cvt_pk_bf16_f32 v103, v78, v79\n\t" \
    "v_mfma_f32_32x32x16_bf16 v[16:31], v[132:135], v[108:111], v[16:31]\n\t" \
    "v_exp_f32 v80, v80\n\t" \
    "v_exp_f32 v81, v81\n\t" \
    "v_exp_f32 v82, v82\n\t" \
    "v_cvt_pk_bf16_f32 v104, v80, v81\n\t" \
    "v_exp_f32 v83, v83\n\t" \
    "v_exp_f32 v84, v84\n\t" \
    "v_cvt_pk_bf16_f32 v105, v82, v83\n\t" \
    "v_exp_f32 v85, v85\n\t" \
    "s_waitcnt lgkmcnt(0)\n\t" \
    "v_mfma_f32_32x32x16_bf16 v[64:79], v[120:123], v[112:115], v[32:47]\n\t" \
    "v_exp_f32 v86, v86\n\t" \
    "v_cvt_pk_bf16_f32 v106, v84, v85\n\t" \
    "v_exp_f32 v87, v87\n\t" \
    "v_exp_f32 v88, v88\n\t" \
    "v_cvt_pk_bf16_f32 v107, v86, v87\n\t" \
    "v_exp_f32 v89, v89\n\t" \
    "v_exp_f32 v90, v90\n\t" \
    "v_cvt_pk_bf16_f32 v108, v88, v89\n\t" \
    "v_mfma_f32_32x32x16_bf16 v[16:31], v[124:127], v[104:107], v[16:31]\n\t" \
    "s_mov_b64 exec, %[vmask]\n\t" \
    "ds_read_b128 v[124:127], v137 offset:6144\n\t" \
    "ds_read_b128 v[132:135], v137 offset:6656\n\t" \
    "s_mov_b64 exec, -1\n\t" \
    "v_exp_f32 v91, v91\n\t" \
    "v_exp_f32 v92, v92\n\t" \
    "v_cvt_pk_bf16_f32 v109, v90, v91\n\t" \
    "v_exp_f32 v93, v93\n\t" \
    "v_exp_f32 v94, v94\n\t" \
    "v_exp_f32 v95, v95\n\t" \
    "v_cvt_pk_bf16_f32 v110, v92, v93\n\t" \
    "v_cvt_pk_bf16_f32 v111, v94, v95\n\t" \
    "v_mfma_f32_32x32x16_bf16 v[0:15], v[128:131], v[100:103], v[0:15]\n\t" \
    "v_exp_f32 v64, v64\n\t" \
    "v_exp_f32 v65, v65\n\t" \
    "v_exp_f32 v66, v66\n\t" \
    "v_cvt_pk_bf16_f32 v96, v64, v65\n\t" \
    "v_exp_f32 v67, v67\n\t" \
    "v_exp_f32 v68, v68\n\t" \
    "v_cvt_pk_bf16_f32 v97, v66, v67\n\t" \
    "v_exp_f32 v69, v69\n\t" \
    "v_mfma_f32_32x32x16_bf16 v[80:95], v[120:123], v[116:119], v[48:63]\n\t" \
    "ds_read_b128 v[120:123], v136 offset:8192\n\t" \
    "v_exp_f32 v70, v70\n\t" \
    "v_cvt_pk_bf16_f32 v98, v68, v69\n\t" \
    "v_exp_f32 v71, v71\n\t" \
    "v_exp_f32 v72, v72\n\t" \
    "v_cvt_pk_bf16_f32 v99, v70, v71\n\t" \
    "v_exp_f32 v73, v73\n\t" \
    "v_exp_f32 v74, v74\n\t" \
    "v_cvt_pk_bf16_f32 v100, v72, v73\n\t" \
    "s_waitcnt lgkmcnt(2)\n\t" \
    "v_mfma_f32_32x32x16_bf16 v[0:15], v[124:127], v[96:99], v[0:15]\n\t" \
    "v_exp_f32 v75, v75\n\t" \
    "v_exp_f32 v76, v76\n\t" \
    "v_cvt_pk_bf16_f32 v101, v74, v75\n\t" \
    "v_exp_f32 v77, v77\n\t" \
    "v_exp_f32 v78, v78\n\t" \
    "v_exp_f32 v79, v79\n\t" \
    "v_cvt_pk_bf16_f32 v102, v76, v77\n\t" \
    "v_cvt_pk_bf16_f32 v103, v78, v79\n\t" \
    "v_mfma_f32_32x32x16_bf16 v[16:31], v[128:131], v[108:111], v[16:31]\n\t" \
    "v_exp_f32 v80, v80\n\t" \
    "v_exp_f32 v81, v81\n\t" \
    "v_exp_f32 v82, v82\n\t" \
    "v_cvt_pk_bf16_f32 v104, v80, v81\n\t" \
    "v_exp_f32 v83, v83\n\t" \
    "v_exp_f32 v84, v84\n\t" \
    "v_cvt_pk_bf16_f32 v105, v82, v83\n\t" \
    "v_exp_f32 v85, v85\n\t" \
    "s_waitcnt lgkmcnt(0)\n\t" \
    "v_mfma_f32_32x32x16_bf16 v[64:79], v[120:123], v[112:115], v[32:47]\n\t" \
    "v_exp_f32 v86, v86\n\t" \
    "v_cvt_pk_bf16_f32 v106, v84, v85\n\t" \
    "v_exp_f32 v87, v87\n\t" \
    "v_exp_f32 v88, v88\n\t" \
    "v_cvt_pk_bf16_f32 v107, v86, v87\n\t" \
    "v_exp_f32 v89, v89\n\t" \
    "v_exp_f32 v90, v90\n\t" \
    "v_cvt_pk_bf16_f32 v108, v88, v89\n\t" \
    "v_mfma_f32_32x32x16_bf16 v[16:31], v[124:127], v[104:107], v[16:31]\n\t" \
    "s_mov_b64 exec, %[vmask]\n\t" \
    "ds_read_b128 v[124:127], v137 offset:8192\n\t" \
    "ds_read_b128 v[128:131], v137 offset:8704\n\t" \
    "s_mov_b64 exec, -1\n\t" \
    "v_exp_f32 v91, v91\n\t" \
    "v_exp_f32 v92, v92\n\t" \
    "v_cvt_pk_bf16_f32 v109, v90, v91\n\t" \
    "v_exp_f32 v93, v93\n\t" \
    "v_exp_f32 v94, v94\n\t" \
    "v_exp_f32 v95, v95\n\t" \
    "v_cvt_pk_bf16_f32 v110, v92, v93\n\t" \
    "v_cvt_pk_bf16_f32 v111, v94, v95\n\t" \
    "v_mfma_f32_32x32x16_bf16 v[0:15], v[132:135], v[100:103], v[0:15]\n\t" \
    "v_add_u32 v136, 0x2000, v136\n\t" \
    "v_add_u32 v137, 0x2000, v137\n\t" \
    "s_cmp_lg_u32 %[cnt], 16\n\t" \
    "s_cbranch_scc1 2f\n\t" \
    "s_waitcnt vmcnt(0)\n\t" \
    "s_mov_b64 exec, 1\n\t" \
    "global_store_dwordx2 v139, v[140:141], %[fbase] sc1\n\t" \
    "s_mov_b64 exec, -1\n\t" \
    "2:\n\t" \
    "s_sub_u32 %[cnt], %[cnt], 1\n\t" \
    "s_cmp_lg_u32 %[cnt], 0\n\t" \
    "s_cbranch_scc1 1b\n\t" \
    "s_nop 1\n\t" \
    "v_mfma_f32_32x32x16_bf16 v[16:31], v[132:135], v[108:111], v[16:31]\n\t" \
    "s_waitcnt lgkmcnt(0)\n\t" \
    "s_nop 15\n\t"

#define ATTN_A_CLOB "v64","v65","v66","v67","v68","v69","v70","v71","v72","v73","v74","v75","v76","v77","v78","v79","v80","v81","v82","v83","v84","v85","v86","v87","v88","v89","v90","v91","v92","v93","v94","v95","v96","v97","v98","v99","v100","v101","v102","v103","v104","v105","v106","v107","v108","v109","v110","v111","v120","v121","v122","v123","v124","v125","v126","v127","v128","v129","v130","v131","v132","v133","v134","v135"

typedef short bf16x8 __attribute__((ext_vector_type(8)));
typedef short s16x4 __attribute__((ext_vector_type(4)));
typedef float f32x16 __attribute__((ext_vector_type(16)));
typedef float f32x2 __attribute__((ext_vector_type(2)));
typedef __bf16 b16x2 __attribute__((ext_vector_type(2)));
typedef unsigned u32x4 __attribute__((ext_vector_type(4)));
typedef unsigned u32x2 __attribute__((ext_vector_type(2)));
typedef unsigned long long u64;
typedef const __attribute__((address_space(3))) char* lds_cptr;
#define LDSP(T) __attribute__((address_space(3))) T*
#define GASP(T) __attribute__((address_space(1))) T*
#define DI __device__ __forceinline__
#define MFMA(a, b, c) __builtin_amdgcn_mfma_f32_32x32x16_bf16(a, b, c, 0, 0, 0)
#define WAIT_VM(n) asm volatile("s_waitcnt vmcnt(" #n ")" ::: "memory")
#define BARRIER() asm volatile("s_waitcnt lgkmcnt(0)\n\ts_barrier" ::: "memory")

constexpr int SEQ = 2048;
constexpr unsigned WS_FQ = 0x1000, WS_FO = 0x8000, WS_Q = 0x100000, WS_KV = 0x500000, WS_O = 0xD00000, WS_BYTES = 0x1100000;
constexpr unsigned Q_BR = 0x200000, KV_BR = 0x400000, Q_HEAD = 0x10000, KV_HEAD = 0x20000;
constexpr float QSCALE = 0.25f * 1.4426950408889634f;
constexpr int STAGE = 32768, LDS_SPARE = 131072, LDS_BYTES = 139264;
constexpr unsigned SPIN_CAP = 1u << 20;
#define RSTAMP(slot) do {} while (0)

DI unsigned cvtpk(float lo, float hi) { f32x2 v = {lo, hi}; b16x2 b = __builtin_convertvector(v, b16x2); return __builtin_bit_cast(unsigned, b); }
template <int SC1> DI void glds16(const void* sbase, unsigned voff, unsigned lds_dst) {
    unsigned keep;
    if (SC1) asm volatile("s_mov_b32 %0, m0\n\ts_mov_b32 m0, %3\n\ts_nop 0\n\tglobal_load_lds_dwordx4 %1, %2 sc1\n\ts_mov_b32 m0, %0" : "=&s"(keep) : "v"(voff), "s"(sbase), "s"(lds_dst) : "memory");
    else asm volatile("s_mov_b32 %0, m0\n\ts_mov_b32 m0, %3\n\ts_nop 0\n\tglobal_load_lds_dwordx4 %1, %2\n\ts_mov_b32 m0, %0" : "=&s"(keep) : "v"(voff), "s"(sbase), "s"(lds_dst) : "memory");
}
DI void touch(const void* sbase, unsigned voff, unsigned lds_dst) {
    unsigned keep;
    asm volatile("s_mov_b32 %0, m0\n\ts_mov_b32 m0, %3\n\ts_nop 0\n\tglobal_load_lds_dword %1, %2\n\ts_mov_b32 m0, %0" : "=&s"(keep) : "v"(voff), "s"(sbase), "s"(lds_dst) : "memory");
}
DI bf16x8 lds_b128(lds_cptr p) { return *(const LDSP(bf16x8))p; }
DI s16x4 lds_tr(lds_cptr p) { return __builtin_amdgcn_ds_read_tr16_b64_v4i16((LDSP(s16x4))p); }
DI bf16x8 cat8(s16x4 a, s16x4 b) { return (bf16x8){a[0], a[1], a[2], a[3], b[0], b[1], b[2], b[3]}; }
DI u32x4 pack8(const f32x16& a, int o, float sc) {
    return (u32x4){cvtpk(a[o] * sc, a[o + 1] * sc), cvtpk(a[o + 2] * sc, a[o + 3] * sc), cvtpk(a[o + 4] * sc, a[o + 5] * sc), cvtpk(a[o + 6] * sc, a[o + 7] * sc)};
}
typedef __amdgpu_buffer_rsrc_t rsrc_t;
DI bf16x8 ld_sc1(rsrc_t r, unsigned off) { return __builtin_bit_cast(bf16x8, __builtin_amdgcn_raw_buffer_load_b128(r, off, 0, 16)); }
DI void st_sc1(rsrc_t r, unsigned off, u32x4 v) { __builtin_amdgcn_raw_buffer_store_b128(v, r, off, 0, 16); }
DI u64 flag_ld(const char* ws, unsigned off) { return __hip_atomic_load((GASP(u64))(ws + off), __ATOMIC_RELAXED, __HIP_MEMORY_SCOPE_AGENT); }
DI void flag_st(char* ws, unsigned off, u64 v) { __hip_atomic_store((GASP(u64))(ws + off), v, __ATOMIC_RELAXED, __HIP_MEMORY_SCOPE_AGENT); }
DI void wait_flags(const char* ws, unsigned off, bool active, u64 tag) {
#pragma unroll 1
    for (unsigned it = 0; it < SPIN_CAP; ++it) {
        const u64 v = active ? flag_ld(ws, off) : tag;
        if (__all(v == tag)) break;
        __builtin_amdgcn_s_sleep(1);
    }
    asm volatile("" ::: "memory");
}
DI float rowmax16(const f32x16& s) {
    float a = fmaxf(fmaxf(s[0], s[1]), s[2]), b = fmaxf(fmaxf(s[3], s[4]), s[5]);
    a = fmaxf(fmaxf(a, s[6]), s[7]); b = fmaxf(fmaxf(b, s[8]), s[9]); a = fmaxf(fmaxf(a, s[10]), s[11]); b = fmaxf(fmaxf(b, s[12]), s[13]); a = fmaxf(fmaxf(a, s[14]), s[15]);
    const float m = fmaxf(a, b); auto rr = __builtin_amdgcn_permlane32_swap(__float_as_uint(m), __float_as_uint(m), false, false);
    return fmaxf(__uint_as_float(rr[0]), __uint_as_float(rr[1]));
}
DI f32x16 splat16(float v) { f32x16 r;
#pragma unroll
    for (int i = 0; i < 16; ++i) r[i] = v; return r; }
DI void exp_pack(f32x16& s, bf16x8& p0, bf16x8& p1) {
#pragma unroll
    for (int i = 0; i < 16; ++i) s[i] = __builtin_amdgcn_exp2f(s[i]);
    p0 = __builtin_bit_cast(bf16x8, pack8(s, 0, 1.f)); p1 = __builtin_bit_cast(bf16x8, pack8(s, 8, 1.f));
}
DI bf16x8 vfix(bf16x8 v, bool lo) { const bf16x8 ones = {0x3F80, 0x3F80, 0x3F80, 0x3F80, 0x3F80, 0x3F80, 0x3F80, 0x3F80}; return lo ? v : ones; }

__global__ __launch_bounds__(512, 2) void fused_kernel(const uint16_t* xA, const uint16_t* wA, const uint16_t* woA, const uint16_t* xB, const uint16_t* wB, const uint16_t* woB, char* ws, uint16_t* out) {
    extern __shared__ __attribute__((aligned(16))) char smem[];
    const int bid = blockIdx.x, xc = bid & 7, j = bid >> 3;
    const int lane = threadIdx.x & 63, wv = __builtin_amdgcn_readfirstlane(threadIdx.x >> 6), hi = lane >> 5, l31 = lane & 31;
    const unsigned lds0 = (unsigned)(uintptr_t)(LDSP(char))smem;
    u64 tag = *(const u64*)ws + 1;
    asm volatile("" : "+s"(tag));
    const rsrc_t rs = __builtin_amdgcn_make_buffer_rsrc(ws, 0, WS_BYTES, 0x00020000);
    RSTAMP(0);
    const int rb = (xc >> 1) * 8 + (j & 7), hg = (xc & 1) * 4 + (j >> 3), c0 = ((j & 7) + 2 * (j >> 3)) & 7;
    const int h = xc * 4 + (j & 3), qb = j >> 2;
    if (wv >= 4) {
        const int lw = wv - 4;
        unsigned voffx[2], voffw[3];
#pragma unroll
        for (int m = 0; m < 2; ++m) voffx[m] = (8 * (2 * lw + m) + (lane >> 3)) * 1024 + (((lane & 7) ^ ((4 * m + (lane >> 4)) & 7)) << 4);
#pragma unroll
        for (int t = 0; t < 3; ++t) { const int off = t * 1024 + lane * 16, r8 = off / 384, c = (off % 384) >> 4; voffw[t] = r8 * 3072 + (c >> 3) * 1024 + hg * 128 + (((c & 7) ^ (((r8 >> 1) & 1) << 2)) << 4); }
        auto stage = [&](int g) {
            const int p = g >> 3, kc = (c0 + g) & 7;
            const char* xb = (const char*)(p ? xB : xA) + (size_t)rb * 64 * 1024 + kc * 128;
            const char* wb = (const char*)(p ? wB : wA) + (size_t)(kc * 64 + 16 * lw) * 3072;
            const unsigned d = lds0 + (g & 3) * STAGE;
#pragma unroll
            for (int m = 0; m < 2; ++m) glds16<0>(xb, voffx[m], d + (2 * lw + m) * 1024);
#pragma unroll
            for (int t = 0; t < 6; ++t) glds16<0>(wb + (t / 3) * 8 * 3072, voffw[t % 3], d + 8192 + (6 * lw + t) * 1024);
        };
        auto kvstage = [&](int s) {
            const char* src = ws + WS_KV + (size_t)h * KV_HEAD + s * STAGE + lw * 8192;
#pragma unroll
            for (int i = 0; i < 8; ++i) glds16<1>(src + i * 1024, lane * 16, lds0 + s * STAGE + lw * 8192 + i * 1024);
        };
        stage(0); stage(1);
        WAIT_VM(8); BARRIER();
        stage(2); stage(3);
        WAIT_VM(16); BARRIER();
#pragma unroll 1
        for (int g = 2; g < 14; ++g) {
            stage(g + 2);
            WAIT_VM(16);
            RSTAMP(2 + g);
            BARRIER();
        }
        WAIT_VM(8); BARRIER();
        WAIT_VM(0); RSTAMP(17); BARRIER();
        BARRIER();
        wait_flags(ws, WS_FQ + (((h >> 1) * 64) + lane) * 8, true, tag);
        kvstage(0); kvstage(1); kvstage(2); kvstage(3);
        WAIT_VM(0); RSTAMP(18);
        BARRIER();
        if (bid == 0 && wv == 7) {
#pragma unroll 1
            for (unsigned it = 0; it < SPIN_CAP; ++it) {
                bool ok = true;
#pragma unroll
                for (int i = 0; i < 16; ++i) ok &= flag_ld(ws, WS_FQ + (i * 64 + lane) * 8) == tag;
                if (__all(ok)) break;
                __builtin_amdgcn_s_sleep(1);
            }
            if (lane == 0) flag_st(ws, 0, tag);
        }
        {
            const int qw = qb * 256 + 64 * lw, T0 = qw / 32 - 8; const bool vlo = l31 < 16;
            { const int t = T0 + lane; wait_flags(ws, WS_FQ + ((16 + (h >> 1)) * 64 + t) * 8, lane < 10 && t >= 0, tag); }
            RSTAMP(19);
            const unsigned kvb = WS_KV + KV_BR + h * KV_HEAD, kofs = kvb + lane * 16, vofs = kvb + 1024 + hi * 256 + (l31 & 15) * 16;
            bf16x8 kf[10];
#pragma unroll
            for (int t = 0; t < 10; ++t) { const int g = T0 + t < 0 ? 0 : T0 + t; kf[t] = ld_sc1(rs, kofs + g * 2048); }
            const unsigned qofs = WS_Q + Q_BR + h * Q_HEAD + (qw / 32) * 1024 + lane * 16;
            const bf16x8 q0f = ld_sc1(rs, qofs), q1f = ld_sc1(rs, qofs + 1024);
            f32x16 mF, mL;
#pragma unroll
            for (int r = 0; r < 16; ++r) { const int jj = (r & 3) + 8 * (r >> 2) + 4 * hi; mF[r] = jj > l31 ? 0.f : -INFINITY; mL[r] = jj <= l31 ? 0.f : -INFINITY; }
#pragma unroll 1
            for (int blk = 0; blk < 2; ++blk) {
                const bf16x8 qf = blk ? q1f : q0f;
                if (blk) {
#pragma unroll
                    for (int t = 0; t < 9; ++t) kf[t] = kf[t + 1]; }
                const int tmin = (T0 + blk < 0) ? -(T0 + blk) : 0;
                bf16x8 v0[9], v1[9];
#pragma unroll
                for (int t = 0; t < 9; ++t) { const int g = T0 + blk + t < 0 ? 0 : T0 + blk + t; v0[t] = ld_sc1(rs, vofs + g * 2048); v1[t] = ld_sc1(rs, vofs + g * 2048 + 512); }
                float m = rowmax16(MFMA(kf[8], qf, mL));
#pragma unroll
                for (int t = 1; t < 8; ++t) if (t >= tmin) m = fmaxf(m, rowmax16(MFMA(kf[t], qf, splat16(0.f))));
                if (tmin == 0) m = fmaxf(m, rowmax16(MFMA(kf[0], qf, mF)));
                f32x16 o = {}; const f32x16 cm = splat16(-m);
#pragma unroll
                for (int t = 0; t <= 8; ++t) if (t >= tmin) {
                    f32x16 c = cm;
                    if (t == 0) {
#pragma unroll
                        for (int r = 0; r < 16; ++r) c[r] += mF[r]; }
                    if (t == 8) {
#pragma unroll
                        for (int r = 0; r < 16; ++r) c[r] += mL[r]; }
                    f32x16 s = MFMA(kf[t], qf, c); bf16x8 p0, p1; exp_pack(s, p0, p1);
                    o = MFMA(vfix(v0[t], vlo), p0, o); o = MFMA(vfix(v1[t], vlo), p1, o);
                }
                st_sc1(rs, WS_O + Q_BR + h * Q_HEAD + (qw / 32 + blk) * 1024 + lane * 16, pack8(o, 0, 1.f / o[8]));
            }
            WAIT_VM(0);
            if (lane == 0) flag_st(ws, WS_FO + ((32 + qw / 64) * 32 + h) * 8, tag);
            RSTAMP(20);
        }
    } else {
        const int tt = wv & 1, hp = wv >> 1, q = (lane >> 2) & 3;
        const int xrow = 32 * tt + l31; const unsigned xaddr0 = xrow * 128 + ((hi ^ ((l31 >> 1) & 7)) << 4);
        const unsigned wbase = 8192 + (8 * hi + q) * 384 + ((hp ^ (q >> 1)) * 64) + ((lane >> 4) & 1) * 32 + (lane & 3) * 8;
        f32x16 acc0, acc1, acc2, fx, fw0, fw1, fw2;
        unsigned xb0 = lds0 + xaddr0, xb1 = lds0 + (xaddr0 ^ 32), xb2 = lds0 + (xaddr0 ^ 64), xb3 = lds0 + (xaddr0 ^ 96), wb_ = lds0 + wbase;
        unsigned soff = 0;
        asm volatile(QKV_FIRST_ASM : "={v[48:63]}"(fx), "={v[64:79]}"(fw0), "={v[80:95]}"(fw1), "={v[96:111]}"(fw2)
                     : [soff] "s"(soff), "{v176}"(xb0), "{v177}"(xb1), "{v178}"(xb2), "{v179}"(xb3), "{v180}"(wb_) : "v181", "v182", "v183", "v184", "v185", "memory");
        RSTAMP(1);
        {
            const unsigned sp = lds0 + LDS_SPARE + wv * 1024;
            if (wv == 0) { const char* xb = (const char*)xB + (size_t)(xc >> 1) * 512 * 1024 + j * 16384; touch(xb, lane * 128, sp); touch(xb + 8192, lane * 128, sp + 256); }
            else if (wv == 1) { const char* wb = (const char*)wB + (xc & 1) * 512;
#pragma unroll
                   for (int i = 0; i < 3; ++i) { const int li = j * 192 + i * 64 + lane, k = li / 12, r = li % 12; touch(wb, k * 3072 + (r >> 2) * 1024 + (r & 3) * 128, sp + i * 256); } }
            else if (lane < 16) touch((const char*)(wv == 2 ? woA : woB) + bid * 2048, lane * 128, sp);
        }
#define QKV_RUN(N) do { int cnt = (N); asm volatile(QKV_PHASE_ASM : "+{v[0:15]}"(acc0), "+{v[16:31]}"(acc1), "+{v[32:47]}"(acc2), "+{v[48:63]}"(fx), "+{v[64:79]}"(fw0), "+{v[80:95]}"(fw1), "+{v[96:111]}"(fw2), [soff] "+s"(soff), [cnt] "+s"(cnt) \
                         : "{v176}"(xb0), "{v177}"(xb1), "{v178}"(xb2), "{v179}"(xb3), "{v180}"(wb_) : QKV_CLOB_B, "scc", "memory"); } while (0)
        const int tile = rb * 2 + tt, h0 = 4 * hg + 2 * hp;
        auto epilogue = [&](int br) {
            const unsigned qo = WS_Q + br * Q_BR + h0 * Q_HEAD + tile * 1024 + lane * 16, ko = WS_KV + br * KV_BR + h0 * KV_HEAD + tile * 2048 + lane * 16;
            st_sc1(rs, ko, pack8(acc1, 0, 1.f)); st_sc1(rs, ko + KV_HEAD, pack8(acc1, 8, 1.f));
            const unsigned vo = WS_KV + br * KV_BR + (h0 + (l31 >> 4)) * KV_HEAD + tile * 2048 + 1024 + hi * 256 + (l31 & 15) * 16;
            st_sc1(rs, vo, pack8(acc2, 0, 1.f)); st_sc1(rs, vo + 512, pack8(acc2, 8, 1.f));
            st_sc1(rs, qo, pack8(acc0, 0, QSCALE)); st_sc1(rs, qo + Q_HEAD, pack8(acc0, 8, QSCALE));
        };
        acc0 = f32x16{}; acc1 = f32x16{}; acc2 = f32x16{};
        QKV_RUN(4);
        RSTAMP(2);
        epilogue(0);
        acc0 = f32x16{}; acc1 = f32x16{}; acc2 = f32x16{};
        QKV_RUN(1);
        WAIT_VM(0);
        if (lane == 0) flag_st(ws, WS_FQ + (((h0 >> 1) * 64) + tile) * 8, tag);
        RSTAMP(3);
        QKV_RUN(3);
        RSTAMP(4);
        epilogue(1);
        BARRIER();
        RSTAMP(5);
        {
            const int qw = qb * 256 + 64 * wv;
            const unsigned qofs = WS_Q + h * Q_HEAD + (qw / 32) * 1024 + lane * 16;
            bf16x8 qa = ld_sc1(rs, qofs), qbf = ld_sc1(rs, qofs + 1024);
            __builtin_amdgcn_s_setprio(2);
            const bf16x8 k0 = lds_b128((lds_cptr)smem + lane * 16);
            f32x16 ca = splat16(-rowmax16(MFMA(k0, qa, splat16(0.f)))), cb = splat16(-rowmax16(MFMA(k0, qbf, splat16(0.f))));
            unsigned kaddr = lds0 + lane * 16, vaddr = lds0 + 1024 + hi * 256 + (l31 & 15) * 16;
            unsigned fill = (l31 == 16 || l31 == 20) ? 0x3f803f80u : 0u;
            unsigned foff = WS_FQ + ((16 + (h0 >> 1)) * 64 + tile) * 8;
            f32x16 oa = {}, ob = {};
            int cnt = 16; const u64 vmask = 0x0000FFFF0000FFFFull;
            RSTAMP(6);
            asm volatile(ATTN_A_ASM
                : "+{v[0:15]}"(oa), "+{v[16:31]}"(ob), [cnt] "+s"(cnt), "+{v136}"(kaddr), "+{v137}"(vaddr)
                : "{v[32:47]}"(ca), "{v[48:63]}"(cb), "{v[112:115]}"(qa), "{v[116:119]}"(qbf), [vmask] "s"(vmask), "{v138}"(fill), "{v139}"(foff), "{v[140:141]}"(tag), [fbase] "s"(ws)
                : ATTN_A_CLOB, "scc", "memory");
            RSTAMP(7);
            const unsigned oo = WS_O + h * Q_HEAD + (qw / 32) * 1024 + lane * 16;
            st_sc1(rs, oo, pack8(oa, 0, 1.f / oa[8])); st_sc1(rs, oo + 1024, pack8(ob, 0, 1.f / ob[8]));
            WAIT_VM(0);
            if (lane == 0) flag_st(ws, WS_FO + ((qw / 64) * 32 + h) * 8, tag);
            __builtin_amdgcn_s_setprio(0);
            RSTAMP(8);
        }
    }
    BARRIER();
    {
        const int br = xc >> 2, orb = (xc & 3) * 4 + (j & 3), cb = j >> 2, tt = wv & 3, kh = wv >> 2;
        const char* wo = (const char*)(br ? woB : woA) + cb * 128;
        const unsigned voff = (lane >> 3) * 1024 + (((lane & 7) ^ (((lane >> 4) & 1) << 2)) << 4);
#pragma unroll
        for (int i = 0; i < 8; ++i) glds16<0>(wo + (wv * 8 + i) * 8192, voff, lds0 + (wv * 8 + i) * 1024);
        const int tile = orb * 4 + tt;
        wait_flags(ws, WS_FO + ((br * 32 + (tile >> 1)) * 32 + kh * 16 + (lane & 15)) * 8, lane < 16, tag);
        RSTAMP(21);
        bf16x8 of[16];
        const unsigned oofs = WS_O + br * Q_BR + kh * 16 * Q_HEAD + tile * 1024 + lane * 16;
#pragma unroll
        for (int i = 0; i < 16; ++i) of[i] = ld_sc1(rs, oofs + i * Q_HEAD);
        const int q = (lane >> 2) & 3;
        unsigned wb[2];
#pragma unroll
        for (int ct = 0; ct < 2; ++ct) wb[ct] = (kh * 256 + 4 * hi + q) * 128 + ((ct ^ (q >> 1)) * 64) + ((lane >> 4) & 1) * 32 + (lane & 3) * 8;
        WAIT_VM(0); BARRIER();
        RSTAMP(22);
        f32x16 acc[2] = {};
#pragma unroll
        for (int i = 0; i < 16; ++i)
#pragma unroll
            for (int ct = 0; ct < 2; ++ct) { lds_cptr p = (lds_cptr)smem + wb[ct] + i * 2048; acc[ct] = MFMA(cat8(lds_tr(p), lds_tr(p + 1024)), of[i], acc[ct]); }
        LDSP(float) xs = (LDSP(float))(smem + 65536) + wv * 1024; LDSP(float) xr = (LDSP(float))(smem + 65536) + (wv ^ 4) * 1024;
#pragma unroll
        for (int r = 0; r < 16; ++r) xs[r * 64 + lane] = kh ? acc[0][r] : acc[1][r];
        BARRIER();
        f32x16 o = kh ? acc[1] : acc[0];
#pragma unroll
        for (int r = 0; r < 16; ++r) o[r] += xr[r * 64 + lane];
        uint16_t* orow = out + (size_t)(tile * 32 + l31) * 1024 + br * 512 + cb * 64 + kh * 32 + 4 * hi;
#pragma unroll
        for (int g = 0; g < 4; ++g) *(u32x2*)(orow + 8 * g) = (u32x2){cvtpk(o[4 * g], o[4 * g + 1]), cvtpk(o[4 * g + 2], o[4 * g + 3])};
        RSTAMP(23);
    }
}

extern "C" void kernel_launch(void* const* d_in, const int* in_sizes, int n_in, void* d_out, int out_size, void* d_ws, size_t ws_size, hipStream_t stream) {
    (void)hipFuncSetAttribute((const void*)fused_kernel, hipFuncAttributeMaxDynamicSharedMemorySize, LDS_BYTES);
    fused_kernel<<<256, 512, LDS_BYTES, stream>>>((const uint16_t*)d_in[0], (const uint16_t*)d_in[1], (const uint16_t*)d_in[2], (const uint16_t*)d_in[3], (const uint16_t*)d_in[4], (const uint16_t*)d_in[5], (char*)d_ws, (uint16_t*)d_out);
}
```

```cpp
#include <hip/hip_runtime.h>
#include <stdint.h>

#define QKV_FIRST_ASM \
    "s_waitcnt lgkmcnt(0)\n\t" \
    "s_barrier\n\t" \
    "v_add_u32 v185, %[soff], v180\n\t" \
    "v_add_u32 v181, %[soff], v176\n\t" \
    "v_add_u32 v182, %[soff], v177\n\t" \
    "v_add_u32 v183, %[soff], v178\n\t" \
    "v_add_u32 v184, %[soff], v179\n\t" \
    "ds_read_b128 v[48:51], v181\n\t" \
    "ds_read_b64_tr_b16 v[64:65], v185 offset:0\n\t" \
    "ds_read_b64_tr_b16 v[66:67], v185 offset:1536\n\t" \
    "ds_read_b64_tr_b16 v[68:69], v185 offset:128\n\t" \
    "ds_read_b64_tr_b16 v[70:71], v185 offset:1664\n\t" \
    "ds_read_b64_tr_b16 v[72:73], v185 offset:256\n\t" \
    "ds_read_b64_tr_b16 v[74:75], v185 offset:1792\n\t" \
    "ds_read_b128 v[52:55], v182\n\t" \
    "ds_read_b64_tr_b16 v[76:77], v185 offset:6144\n\t" \
    "ds_read_b64_tr_b16 v[78:79], v185 offset:7680\n\t" \
    "ds_read_b64_tr_b16 v[80:81], v185 offset:6272\n\t" \
    "ds_read_b64_tr_b16 v[82:83], v185 offset:7808\n\t" \
    "ds_read_b64_tr_b16 v[84:85], v185 offset:6400\n\t" \
    "ds_read_b64_tr_b16 v[86:87], v185 offset:7936\n\t" \
    "ds_read_b128 v[56:59], v183\n\t" \
    "ds_read_b64_tr_b16 v[88:89], v185 offset:12288\n\t" \
    "ds_read_b64_tr_b16 v[90:91], v185 offset:13824\n\t" \
    "ds_read_b64_tr_b16 v[92:93], v185 offset:12416\n\t" \
    "ds_read_b64_tr_b16 v[94:95], v185 offset:13952\n\t" \
    "ds_read_b64_tr_b16 v[96:97], v185 offset:12544\n\t" \
    "ds_read_b64_tr_b16 v[98:99], v185 offset:14080\n\t" \
    "ds_read_b128 v[60:63], v184\n\t" \
    "ds_read_b64_tr_b16 v[100:101], v185 offset:18432\n\t" \
    "ds_read_b64_tr_b16 v[102:103], v185 offset:19968\n\t" \
    "ds_read_b64_tr_b16 v[104:105], v185 offset:18560\n\t" \
    "ds_read_b64_tr_b16 v[106:107], v185 offset:20096\n\t" \
    "ds_read_b64_tr_b16 v[108:109], v185 offset:18688\n\t" \
    "ds_read_b64_tr_b16 v[110:111], v185 offset:20224\n\t" \
    "s_waitcnt lgkmcnt(0)\n\t"

#define QKV_STEP_AB \
    "s_waitcnt lgkmcnt(0)\n\t" \
    "s_barrier\n\t" \
    "v_mfma_f32_32x32x16_bf16 v[0:15], v[64:67], v[48:51], v[0:15]\n\t" \
    "s_add_u32 %[soff], %[soff], 0x8000\n\t" \
    "s_cmp_eq_u32 %[soff], 0x28000\n\t" \
    "s_cselect_b32 %[soff], 0, %[soff]\n\t" \
    "v_add_u32 v185, %[soff], v180\n\t" \
    "v_add_u32 v181, %[soff], v176\n\t" \
    "v_add_u32 v182, %[soff], v177\n\t" \
    "v_add_u32 v183, %[soff], v178\n\t" \
    "v_add_u32 v184, %[soff], v179\n\t" \
    "ds_read_b128 v[112:115], v181\n\t" \
    "ds_read_b64_tr_b16 v[128:129], v185 offset:0\n\t" \
    "ds_read_b64_tr_b16 v[130:131], v185 offset:1536\n\t" \
    "v_mfma_f32_32x32x16_bf16 v[16:31], v[68:71], v[48:51], v[16:31]\n\t" \
    "ds_read_b64_tr_b16 v[132:133], v185 offset:128\n\t" \
    "ds_read_b64_tr_b16 v[134:135], v185 offset:1664\n\t" \
    "ds_read_b64_tr_b16 v[136:137], v185 offset:256\n\t" \
    "v_mfma_f32_32x32x16_bf16 v[32:47], v[48:51], v[72:75], v[32:47]\n\t" \
    "ds_read_b64_tr_b16 v[138:139], v185 offset:1792\n\t" \
    "ds_read_b128 v[116:119], v182\n\t" \
    "ds_read_b64_tr_b16 v[140:141], v185 offset:6144\n\t" \
    "v_mfma_f32_32x32x16_bf16 v[0:15], v[76:79], v[52:55], v[0:15]\n\t" \
    "ds_read_b64_tr_b16 v[142:143], v185 offset:7680\n\t" \
    "ds_read_b64_tr_b16 v[144:145], v185 offset:6272\n\t" \
    "ds_read_b64_tr_b16 v[146:147], v185 offset:7808\n\t" \
    "v_mfma_f32_32x32x16_bf16 v[16:31], v[80:83], v[52:55], v[16:31]\n\t" \
    "ds_read_b64_tr_b16 v[148:149], v185 offset:6400\n\t" \
    "ds_read_b64_tr_b16 v[150:151], v185 offset:7936\n\t" \
    "ds_read_b128 v[120:123], v183\n\t" \
    "v_mfma_f32_32x32x16_bf16 v[32:47], v[52:55], v[84:87], v[32:47]\n\t" \
    "ds_read_b64_tr_b16 v[152:153], v185 offset:12288\n\t" \
    "ds_read_b64_tr_b16 v[154:155], v185 offset:13824\n\t" \
    "ds_read_b64_tr_b16 v[156:157], v185 offset:12416\n\t" \
    "v_mfma_f32_32x32x16_bf16 v[0:15], v[88:91], v[56:59], v[0:15]\n\t" \
    "ds_read_b64_tr_b16 v[158:159], v185 offset:13952\n\t" \
    "ds_read_b64_tr_b16 v[160:161], v185 offset:12544\n\t" \
    "ds_read_b64_tr_b16 v[162:163], v185 offset:14080\n\t" \
    "v_mfma_f32_32x32x16_bf16 v[16:31], v[92:95], v[56:59], v[16:31]\n\t" \
    "ds_read_b128 v[124:127], v184\n\t" \
    "ds_read_b64_tr_b16 v[164:165], v185 offset:18432\n\t" \
    "v_mfma_f32_32x32x16_bf16 v[32:47], v[56:59], v[96:99], v[32:47]\n\t" \
    "ds_read_b64_tr_b16 v[166:167], v185 offset:19968\n\t" \
    "ds_read_b64_tr_b16 v[168:169], v185 offset:18560\n\t" \
    "v_mfma_f32_32x32x16_bf16 v[0:15], v[100:103], v[60:63], v[0:15]\n\t" \
    "ds_read_b64_tr_b16 v[170:171], v185 offset:20096\n\t" \
    "ds_read_b64_tr_b16 v[172:173], v185 offset:18688\n\t" \
    "v_mfma_f32_32x32x16_bf16 v[16:31], v[104:107], v[60:63], v[16:31]\n\t" \
    "ds_read_b64_tr_b16 v[174:175], v185 offset:20224\n\t" \
    "v_mfma_f32_32x32x16_bf16 v[32:47], v[60:63], v[108:111], v[32:47]\n\t"

#define QKV_STEP_BA \
    "s_waitcnt lgkmcnt(0)\n\t" \
    "s_barrier\n\t" \
    "v_mfma_f32_32x32x16_bf16 v[0:15], v[128:131], v[112:115], v[0:15]\n\t" \
    "s_add_u32 %[soff], %[soff], 0x8000\n\t" \
    "s_cmp_eq_u32 %[soff], 0x28000\n\t" \
    "s_cselect_b32 %[soff], 0, %[soff]\n\t" \
    "v_add_u32 v185, %[soff], v180\n\t" \
    "v_add_u32 v181, %[soff], v176\n\t" \
    "v_add_u32 v182, %[soff], v177\n\t" \
    "v_add_u32 v183, %[soff], v178\n\t" \
    "v_add_u32 v184, %[soff], v179\n\t" \
    "ds_read_b128 v[48:51], v181\n\t" \
    "ds_read_b64_tr_b16 v[64:65], v185 offset:0\n\t" \
    "ds_read_b64_tr_b16 v[66:67], v185 offset:1536\n\t" \
    "v_mfma_f32_32x32x16_bf16 v[16:31], v[132:135], v[112:115], v[16:31]\n\t" \
    "ds_read_b64_tr_b16 v[68:69], v185 offset:128\n\t" \
    "ds_read_b64_tr_b16 v[70:71], v185 offset:1664\n\t" \
    "ds_read_b64_tr_b16 v[72:73], v185 offset:256\n\t" \
    "v_mfma_f32_32x32x16_bf16 v[32:47], v[112:115], v[136:139], v[32:47]\n\t" \
    "ds_read_b64_tr_b16 v[74:75], v185 offset:1792\n\t" \
    "ds_read_b128 v[52:55], v182\n\t" \
    "ds_read_b64_tr_b16 v[76:77], v185 offset:6144\n\t" \
    "v_mfma_f32_32x32x16_bf16 v[0:15], v[140:143], v[116:119], v[0:15]\n\t" \
    "ds_read_b64_tr_b16 v[78:79], v185 offset:7680\n\t" \
    "ds_read_b64_tr_b16 v[80:81], v185 offset:6272\n\t" \
    "ds_read_b64_tr_b16 v[82:83], v185 offset:7808\n\t" \
    "v_mfma_f32_32x32x16_bf16 v[16:31], v[144:147], v[116:119], v[16:31]\n\t" \
    "ds_read_b64_tr_b16 v[84:85], v185 offset:6400\n\t" \
    "ds_read_b64_tr_b16 v[86:87], v185 offset:7936\n\t" \
    "ds_read_b128 v[56:59], v183\n\t" \
    "v_mfma_f32_32x32x16_bf16 v[32:47], v[116:119], v[148:151], v[32:47]\n\t" \
    "ds_read_b64_tr_b16 v[88:89], v185 offset:12288\n\t" \
    "ds_read_b64_tr_b16 v[90:91], v185 offset:13824\n\t" \
    "ds_read_b64_tr_b16 v[92:93], v185 offset:12416\n\t" \
    "v_mfma_f32_32x32x16_bf16 v[0:15], v[152:155], v[120:123], v[0:15]\n\t" \
    "ds_read_b64_tr_b16 v[94:95], v185 offset:13952\n\t" \
    "ds_read_b64_tr_b16 v[96:97], v185 offset:12544\n\t" \
    "ds_read_b64_tr_b16 v[98:99], v185 offset:14080\n\t" \
    "v_mfma_f32_32x32x16_bf16 v[16:31], v[156:159], v[120:123], v[16:31]\n\t" \
    "ds_read_b128 v[60:63], v184\n\t" \
    "ds_read_b64_tr_b16 v[100:101], v185 offset:18432\n\t" \
    "v_mfma_f32_32x32x16_bf16 v[32:47], v[120:123], v[160:163], v[32:47]\n\t" \
    "ds_read_b64_tr_b16 v[102:103], v185 offset:19968\n\t" \
    "ds_read_b64_tr_b16 v[104:105], v185 offset:18560\n\t" \
    "v_mfma_f32_32x32x16_bf16 v[0:15], v[164:167], v[124:127], v[0:15]\n\t" \
    "ds_read_b64_tr_b16 v[106:107], v185 offset:20096\n\t" \
    "ds_read_b64_tr_b16 v[108:109], v185 offset:18688\n\t" \
    "v_mfma_f32_32x32x16_bf16 v[16:31], v[168:171], v[124:127], v[16:31]\n\t" \
    "ds_read_b64_tr_b16 v[110:111], v185 offset:20224\n\t" \
    "v_mfma_f32_32x32x16_bf16 v[32:47], v[124:127], v[172:175], v[32:47]\n\t"

#define ATTN_A_ASM \
    "v_mov_b32 v124, v138\n\t" \
    "v_mov_b32 v125, v138\n\t" \
    "v_mov_b32 v126, v138\n\t" \
    "v_mov_b32 v127, v138\n\t" \
    "v_mov_b32 v128, v138\n\t" \
    "v_mov_b32 v129, v138\n\t" \
    "v_mov_b32 v130, v138\n\t" \
    "v_mov_b32 v131, v138\n\t" \
    "v_mov_b32 v132, v138\n\t" \
    "v_mov_b32 v133, v138\n\t" \
    "v_mov_b32 v134, v138\n\t" \
    "v_mov_b32 v135, v138\n\t" \
    "v_mov_b32 v108, 0\n\t" \
    "v_mov_b32 v109, 0\n\t" \
    "v_mov_b32 v110, 0\n\t" \
    "v_mov_b32 v111, 0\n\t" \
    "ds_read_b128 v[120:123], v136\n\t" \
    "s_mov_b64 exec, %[vmask]\n\t" \
    "ds_read_b128 v[124:127], v137\n\t" \
    "ds_read_b128 v[128:131], v137 offset:512\n\t" \
    "s_mov_b64 exec, -1\n\t" \
    "s_waitcnt lgkmcnt(2)\n\t" \
    "v_mfma_f32_32x32x16_bf16 v[64:79], v[120:123], v[112:115], v[32:47]\n\t" \
    "s_nop 15\n\t" \
    "1:\n\t" \
    "v_exp_f32 v64, v64\n\t" \
    "v_exp_f32 v65, v65\n\t" \
    "v_exp_f32 v66, v66\n\t" \
    "v_cvt_pk_bf16_f32 v96, v64, v65\n\t" \
    "v_exp_f32 v67, v67\n\t" \
    "v_exp_f32 v68, v68\n\t" \
    "v_cvt_pk_bf16_f32 v97, v66, v67\n\t" \
    "v_exp_f32 v69, v69\n\t" \
    "v_mfma_f32_32x32x16_bf16 v[80:95], v[120:123], v[116:119], v[48:63]\n\t" \
    "ds_read_b128 v[120:123], v136 offset:2048\n\t" \
    "v_exp_f32 v70, v70\n\t" \
    "v_cvt_pk_bf16_f32 v98, v68, v69\n\t" \
    "v_exp_f32 v71, v71\n\t" \
    "v_exp_f32 v72, v72\n\t" \
    "v_cvt_pk_bf16_f32 v99, v70, v71\n\t" \
    "v_exp_f32 v73, v73\n\t" \
    "v_exp_f32 v74, v74\n\t" \
    "v_cvt_pk_bf16_f32 v100, v72, v73\n\t" \
    "s_waitcnt lgkmcnt(2)\n\t" \
    "v_mfma_f32_32x32x16_bf16 v[0:15], v[124:127], v[96:99], v[0:15]\n\t" \
    "v_exp_f32 v75, v75\n\t" \
    "v_exp_f32 v76, v76\n\t" \
    "v_cvt_pk_bf16_f32 v101, v74, v75\n\t" \
    "v_exp_f32 v77, v77\n\t" \
    "v_exp_f32 v78, v78\n\t" \
    "v_exp_f32 v79, v79\n\t" \
    "v_cvt_pk_bf16_f32 v102, v76, v77\n\t" \
    "v_cvt_pk_bf16_f32 v103, v78, v79\n\t" \
    "v_mfma_f32_32x32x16_bf16 v[16:31], v[132:135], v[108:111], v[16:31]\n\t" \
    "v_exp_f32 v80, v80\n\t" \
    "v_exp_f32 v81, v81\n\t" \
    "v_exp_f32 v82, v82\n\t" \
    "v_cvt_pk_bf16_f32 v104, v80, v81\n\t" \
    "v_exp_f32 v83, v83\n\t" \
    "v_exp_f32 v84, v84\n\t" \
    "v_cvt_pk_bf16_f32 v105, v82, v83\n\t" \
    "v_exp_f32 v85, v85\n\t" \
    "s_waitcnt lgkmcnt(0)\n\t" \
    "v_mfma_f32_32x32x16_bf16 v[64:79], v[120:123], v[112:115], v[32:47]\n\t" \
    "v_exp_f32 v86, v86\n\t" \
    "v_cvt_pk_bf16_f32 v106, v84, v85\n\t" \
    "v_exp_f32 v87, v87\n\t" \
    "v_exp_f32 v88, v88\n\t" \
    "v_cvt_pk_bf16_f32 v107, v86, v87\n\t" \
    "v_exp_f32 v89, v89\n\t" \
    "v_exp_f32 v90, v90\n\t" \
    "v_cvt_pk_bf16_f32 v108, v88, v89\n\t" \
    "v_mfma_f32_32x32x16_bf16 v[16:31], v[124:127], v[104:107], v[16:31]\n\t" \
    "s_mov_b64 exec, %[vmask]\n\t" \
    "ds_read_b128 v[124:127], v137 offset:2048\n\t" \
    "ds_read_b128 v[132:135], v137 offset:2560\n\t" \
    "s_mov_b64 exec, -1\n\t" \
    "v_exp_f32 v91, v91\n\t" \
    "v_exp_f32 v92, v92\n\t" \
    "v_cvt_pk_bf16_f32 v109, v90, v91\n\t" \
    "v_exp_f32 v93, v93\n\t" \
    "v_exp_f32 v94, v94\n\t" \
    "v_exp_f32 v95, v95\n\t" \
    "v_cvt_pk_bf16_f32 v110, v92, v93\n\t" \
    "v_cvt_pk_bf16_f32 v111, v94, v95\n\t" \
    "v_mfma_f32_32x32x16_bf16 v[0:15], v[128:131], v[100:103], v[0:15]\n\t" \
    "v_exp_f32 v64, v64\n\t" \
    "v_exp_f32 v65, v65\n\t" \
    "v_exp_f32 v66, v66\n\t" \
    "v_cvt_pk_bf16_f32 v96, v64, v65\n\t" \
    "v_exp_f32 v67, v67\n\t" \
    "v_exp_f32 v68, v68\n\t" \
    "v_cvt_pk_bf16_f32 v97, v66, v67\n\t" \
    "v_exp_f32 v69, v69\n\t" \
    "v_mfma_f32_32x32x16_bf16 v[80:95], v[120:123], v[116:119], v[48:63]\n\t" \
    "ds_read_b128 v[120:123], v136 offset:4096\n\t" \
    "v_exp_f32 v70, v70\n\t" \
    "v_cvt_pk_bf16_f32 v98, v68, v69\n\t" \
    "v_exp_f32 v71, v71\n\t" \
    "v_exp_f32 v72, v72\n\t" \
    "v_cvt_pk_bf16_f32 v99, v70, v71\n\t" \
    "v_exp_f32 v73, v73\n\t" \
    "v_exp_f32 v74, v74\n\t" \
    "v_cvt_pk_bf16_f32 v100, v72, v73\n\t" \
    "s_waitcnt lgkmcnt(2)\n\t" \
    "v_mfma_f32_32x32x16_bf16 v[0:15], v[124:127], v[96:99], v[0:15]\n\t" \
    "v_exp_f32 v75, v75\n\t" \
    "v_exp_f32 v76, v76\n\t" \
    "v_cvt_pk_bf16_f32 v101, v74, v75\n\t" \
    "v_exp_f32 v77, v77\n\t" \
    "v_exp_f32 v78, v78\n\t" \
    "v_exp_f32 v79, v79\n\t" \
    "v_cvt_pk_bf16_f32 v102, v76, v77\n\t" \
    "v_cvt_pk_bf16_f32 v103, v78, v79\n\t" \
    "v_mfma_f32_32x32x16_bf16 v[16:31], v[128:131], v[108:111], v[16:31]\n\t" \
    "v_exp_f32 v80, v80\n\t" \
    "v_exp_f32 v81, v81\n\t" \
    "v_exp_f32 v82, v82\n\t" \
    "v_cvt_pk_bf16_f32 v104, v80, v81\n\t" \
    "v_exp_f32 v83, v83\n\t" \
    "v_exp_f32 v84, v84\n\t" \
    "v_cvt_pk_bf16_f32 v105, v82, v83\n\t" \
    "v_exp_f32 v85, v85\n\t" \
    "s_waitcnt lgkmcnt(0)\n\t" \
    "v_mfma_f32_32x32x16_bf16 v[64:79], v[120:123], v[112:115], v[32:47]\n\t" \
    "v_exp_f32 v86, v86\n\t" \
    "v_cvt_pk_bf16_f32 v106, v84, v85\n\t" \
    "v_exp_f32 v87, v87\n\t" \
    "v_exp_f32 v88, v88\n\t" \
    "v_cvt_pk_bf16_f32 v107, v86, v87\n\t" \
    "v_exp_f32 v89, v89\n\t" \
    "v_exp_f32 v90, v90\n\t" \
    "v_cvt_pk_bf16_f32 v108, v88, v89\n\t" \
    "v_mfma_f32_32x32x16_bf16 v[16:31], v[124:127], v[104:107], v[16:31]\n\t" \
    "s_mov_b64 exec, %[vmask]\n\t" \
    "ds_read_b128 v[124:127], v137 offset:4096\n\t" \
    "ds_read_b128 v[128:131], v137 offset:4608\n\t" \
    "s_mov_b64 exec, -1\n\t" \
    "v_exp_f32 v91, v91\n\t" \
    "v_exp_f32 v92, v92\n\t" \
    "v_cvt_pk_bf16_f32 v109, v90, v91\n\t" \
    "v_exp_f32 v93, v93\n\t" \
    "v_exp_f32 v94, v94\n\t" \
    "v_exp_f32 v95, v95\n\t" \
    "v_cvt_pk_bf16_f32 v110, v92, v93\n\t" \
    "v_cvt_pk_bf16_f32 v111, v94, v95\n\t" \
    "v_mfma_f32_32x32x16_bf16 v[0:15], v[132:135], v[100:103], v[0:15]\n\t" \
    "v_exp_f32 v64, v64\n\t" \
    "v_exp_f32 v65, v65\n\t" \
    "v_exp_f32 v66, v66\n\t" \
    "v_cvt_pk_bf16_f32 v96, v64, v65\n\t" \
    "v_exp_f32 v67, v67\n\t" \
    "v_exp_f32 v68, v68\n\t" \
    "v_cvt_pk_bf16_f32 v97, v66, v67\n\t" \
    "v_exp_f32 v69, v69\n\t" \
    "v_mfma_f32_32x32x16_bf16 v[80:95], v[120:123], v[116:119], v[48:63]\n\t" \
    "ds_read_b128 v[120:123], v136 offset:6144\n\t" \
    "v_exp_f32 v70, v70\n\t" \
    "v_cvt_pk_bf16_f32 v98, v68, v69\n\t" \
    "v_exp_f32 v71, v71\n\t" \
    "v_exp_f32 v72, v72\n\t" \
    "v_cvt_pk_bf16_f32 v99, v70, v71\n\t" \
    "v_exp_f32 v73, v73\n\t" \
    "v_exp_f32 v74, v74\n\t" \
    "v_cvt_pk_bf16_f32 v100, v72, v73\n\t" \
    "s_waitcnt lgkmcnt(2)\n\t" \
    "v_mfma_f32_32x32x16_bf16 v[0:15], v[124:127], v[96:99], v[0:15]\n\t" \
    "v_exp_f32 v75, v75\n\t" \
    "v_exp_f32 v76, v76\n\t" \
    "v_cvt_pk_bf16_f32 v101, v74, v75\n\t" \
    "v_exp_f32 v77, v77\n\t" \
    "v_exp_f32 v78, v78\n\t" \
    "v_exp_f32 v79, v79\n\t" \
    "v_cvt_pk_bf16_f32 v102, v76, v77\n\t" \
    "v_cvt_pk_bf16_f32 v103, v78, v79\n\t" \
    "v_mfma_f32_32x32x16_bf16 v[16:31], v[132:135], v[108:111], v[16:31]\n\t" \
    "v_exp_f32 v80, v80\n\t" \
    "v_exp_f32 v81, v81\n\t" \
    "v_exp_f32 v82, v82\n\t" \
    "v_cvt_pk_bf16_f32 v104, v80, v81\n\t" \
    "v_exp_f32 v83, v83\n\t" \
    "v_exp_f32 v84, v84\n\t" \
    "v_cvt_pk_bf16_f32 v105, v82, v83\n\t" \
    "v_exp_f32 v85, v85\n\t" \
    "s_waitcnt lgkmcnt(0)\n\t" \
    "v_mfma_f32_32x32x16_bf16 v[64:79], v[120:123], v[112:115], v[32:47]\n\t" \
    "v_exp_f32 v86, v86\n\t" \
    "v_cvt_pk_bf16_f32 v106, v84, v85\n\t" \
    "v_exp_f32 v87, v87\n\t" \
    "v_exp_f32 v88, v88\n\t" \
    "v_cvt_pk_bf16_f32 v107, v86, v87\n\t" \
    "v_exp_f32 v89, v89\n\t" \
    "v_exp_f32 v90, v90\n\t" \
    "v_cvt_pk_bf16_f32 v108, v88, v89\n\t" \
    "v_mfma_f32_32x32x16_bf16 v[16:31], v[124:127], v[104:107], v[16:31]\n\t" \
    "s_mov_b64 exec, %[vmask]\n\t" \
    "ds_read_b128 v[124:127], v137 offset:6144\n\t" \
    "ds_read_b128 v[132:135], v137 offset:6656\n\t" \
    "s_mov_b64 exec, -1\n\t" \
    "v_exp_f32 v91, v91\n\t" \
    "v_exp_f32 v92, v92\n\t" \
    "v_cvt_pk_bf16_f32 v109, v90, v91\n\t" \
    "v_exp_f32 v93, v93\n\t" \
    "v_exp_f32 v94, v94\n\t" \
    "v_exp_f32 v95, v95\n\t" \
    "v_cvt_pk_bf16_f32 v110, v92, v93\n\t" \
    "v_cvt_pk_bf16_f32 v111, v94, v95\n\t" \
    "v_mfma_f32_32x32x16_bf16 v[0:15], v[128:131], v[100:103], v[0:15]\n\t" \
    "v_exp_f32 v64, v64\n\t" \
    "v_exp_f32 v65, v65\n\t" \
    "v_exp_f32 v66, v66\n\t" \
    "v_cvt_pk_bf16_f32 v96, v64, v65\n\t" \
    "v_exp_f32 v67, v67\n\t" \
    "v_exp_f32 v68, v68\n\t" \
    "v_cvt_pk_bf16_f32 v97, v66, v67\n\t" \
    "v_exp_f32 v69, v69\n\t" \
    "v_mfma_f32_32x32x16_bf16 v[80:95], v[120:123], v[116:119], v[48:63]\n\t" \
    "ds_read_b128 v[120:123], v136 offset:8192\n\t" \
    "v_exp_f32 v70, v70\n\t" \
    "v_cvt_pk_bf16_f32 v98, v68, v69\n\t" \
    "v_exp_f32 v71, v71\n\t" \
    "v_exp_f32 v72, v72\n\t" \
    "v_cvt_pk_bf16_f32 v99, v70, v71\n\t" \
    "v_exp_f32 v73, v73\n\t" \
    "v_exp_f32 v74, v74\n\t" \
    "v_cvt_pk_bf16_f32 v100, v72, v73\n\t" \
    "s_waitcnt lgkmcnt(2)\n\t" \
    "v_mfma_f32_32x32x16_bf16 v[0:15], v[124:127], v[96:99], v[0:15]\n\t" \
    "v_exp_f32 v75, v75\n\t" \
    "v_exp_f32 v76, v76\n\t" \
    "v_cvt_pk_bf16_f32 v101, v74, v75\n\t" \
    "v_exp_f32 v77, v77\n\t" \
    "v_exp_f32 v78, v78\n\t" \
    "v_exp_f32 v79, v79\n\t" \
    "v_cvt_pk_bf16_f32 v102, v76, v77\n\t" \
    "v_cvt_pk_bf16_f32 v103, v78, v79\n\t" \
    "v_mfma_f32_32x32x16_bf16 v[16:31], v[128:131], v[108:111], v[16:31]\n\t" \
    "v_exp_f32 v80, v80\n\t" \
    "v_exp_f32 v81, v81\n\t" \
    "v_exp_f32 v82, v82\n\t" \
    "v_cvt_pk_bf16_f32 v104, v80, v81\n\t" \
    "v_exp_f32 v83, v83\n\t" \
    "v_exp_f32 v84, v84\n\t" \
    "v_cvt_pk_bf16_f32 v105, v82, v83\n\t" \
    "v_exp_f32 v85, v85\n\t" \
    "s_waitcnt lgkmcnt(0)\n\t" \
    "v_mfma_f32_32x32x16_bf16 v[64:79], v[120:123], v[112:115], v[32:47]\n\t" \
    "v_exp_f32 v86, v86\n\t" \
    "v_cvt_pk_bf16_f32 v106, v84, v85\n\t" \
    "v_exp_f32 v87, v87\n\t" \
    "v_exp_f32 v88, v88\n\t" \
    "v_cvt_pk_bf16_f32 v107, v86, v87\n\t" \
    "v_exp_f32 v89, v89\n\t" \
    "v_exp_f32 v90, v90\n\t" \
    "v_cvt_pk_bf16_f32 v108, v88, v89\n\t" \
    "v_mfma_f32_32x32x16_bf16 v[16:31], v[124:127], v[104:107], v[16:31]\n\t" \
    "s_mov_b64 exec, %[vmask]\n\t" \
    "ds_read_b128 v[124:127], v137 offset:8192\n\t" \
    "ds_read_b128 v[128:131], v137 offset:8704\n\t" \
    "s_mov_b64 exec, -1\n\t" \
    "v_exp_f32 v91, v91\n\t" \
    "v_exp_f32 v92, v92\n\t" \
    "v_cvt_pk_bf16_f32 v109, v90, v91\n\t" \
    "v_exp_f32 v93, v93\n\t" \
    "v_exp_f32 v94, v94\n\t" \
    "v_exp_f32 v95, v95\n\t" \
    "v_cvt_pk_bf16_f32 v110, v92, v93\n\t" \
    "v_cvt_pk_bf16_f32 v111, v94, v95\n\t" \
    "v_mfma_f32_32x32x16_bf16 v[0:15], v[132:135], v[100:103], v[0:15]\n\t" \
    "v_add_u32 v136, 0x2000, v136\n\t" \
    "v_add_u32 v137, 0x2000, v137\n\t" \
    "s_cmp_lg_u32 %[cnt], 16\n\t" \
    "s_cbranch_scc1 2f\n\t" \
    "s_waitcnt vmcnt(0)\n\t" \
    "s_mov_b64 exec, 1\n\t" \
    "global_store_dwordx2 v139, v[140:141], %[fbase] sc1\n\t" \
    "s_mov_b64 exec, -1\n\t" \
    "2:\n\t" \
    "s_cmp_lg_u32 %[cnt], 15\n\t" \
    "s_cbranch_scc1 4f\n\t" \
    "s_barrier\n\t" \
    "4:\n\t" \
    "s_cmp_lg_u32 %[cnt], 8\n\t" \
    "s_cbranch_scc1 5f\n\t" \
    "ds_write_b32 v142, v143\n\t" \
    "s_waitcnt lgkmcnt(0)\n\t" \
    "5:\n\t" \
    "s_sub_u32 %[cnt], %[cnt], 1\n\t" \
    "s_cmp_lg_u32 %[cnt], 0\n\t" \
    "s_cbranch_scc1 1b\n\t" \
    "s_nop 1\n\t" \
    "v_mfma_f32_32x32x16_bf16 v[16:31], v[132:135], v[108:111], v[16:31]\n\t" \
    "s_waitcnt lgkmcnt(0)\n\t" \
    "s_nop 15\n\t"

#define ATTN_B_ASM \
    "v_mov_b32 v124, v162\n\t" \
    "v_mov_b32 v125, v162\n\t" \
    "v_mov_b32 v126, v162\n\t" \
    "v_mov_b32 v127, v162\n\t" \
    "v_mov_b32 v128, v162\n\t" \
    "v_mov_b32 v129, v162\n\t" \
    "v_mov_b32 v130, v162\n\t" \
    "v_mov_b32 v131, v162\n\t" \
    "v_mov_b32 v136, v162\n\t" \
    "v_mov_b32 v137, v162\n\t" \
    "v_mov_b32 v138, v162\n\t" \
    "v_mov_b32 v139, v162\n\t" \
    "v_mov_b32 v140, v162\n\t" \
    "v_mov_b32 v141, v162\n\t" \
    "v_mov_b32 v142, v162\n\t" \
    "v_mov_b32 v143, v162\n\t" \
    "v_mov_b32 v108, 0\n\t" \
    "v_mov_b32 v109, 0\n\t" \
    "v_mov_b32 v110, 0\n\t" \
    "v_mov_b32 v111, 0\n\t" \
    "ds_read_b128 v[120:123], v160\n\t" \
    "s_mov_b64 exec, %[vmask]\n\t" \
    "ds_read_b128 v[124:127], v161\n\t" \
    "ds_read_b128 v[128:131], v161 offset:512\n\t" \
    "s_mov_b64 exec, -1\n\t" \
    "ds_read_b128 v[132:135], v160 offset:2048\n\t" \
    "s_mov_b64 exec, %[vmask]\n\t" \
    "ds_read_b128 v[136:139], v161 offset:2048\n\t" \
    "ds_read_b128 v[140:143], v161 offset:2560\n\t" \
    "s_mov_b64 exec, -1\n\t" \
    "s_waitcnt lgkmcnt(3)\n\t" \
    "v_mfma_f32_32x32x16_bf16 v[64:79], v[120:123], v[112:115], v[32:47]\n\t" \
    "s_nop 15\n\t" \
    "ds_read_b128 v[120:123], v160 offset:4096\n\t" \
    "v_exp_f32 v64, v64\n\t" \
    "v_exp_f32 v65, v65\n\t" \
    "v_exp_f32 v66, v66\n\t" \
    "v_cvt_pk_bf16_f32 v96, v64, v65\n\t" \
    "v_exp_f32 v67, v67\n\t" \
    "v_exp_f32 v68, v68\n\t" \
    "v_cvt_pk_bf16_f32 v97, v66, v67\n\t" \
    "v_exp_f32 v69, v69\n\t" \
    "s_waitcnt lgkmcnt(3)\n\t" \
    "v_mfma_f32_32x32x16_bf16 v[80:95], v[132:135], v[116:119], v[48:63]\n\t" \
    "v_exp_f32 v70, v70\n\t" \
    "v_cvt_pk_bf16_f32 v98, v68, v69\n\t" \
    "v_exp_f32 v71, v71\n\t" \
    "v_exp_f32 v72, v72\n\t" \
    "v_cvt_pk_bf16_f32 v99, v70, v71\n\t" \
    "v_exp_f32 v73, v73\n\t" \
    "v_exp_f32 v74, v74\n\t" \
    "v_cvt_pk_bf16_f32 v100, v72, v73\n\t" \
    "v_and_b32 v96, v96, v144\n\t" \
    "v_and_b32 v97, v97, v145\n\t" \
    "v_and_b32 v98, v98, v146\n\t" \
    "v_and_b32 v99, v99, v147\n\t" \
    "s_nop 1\n\t" \
    "v_mfma_f32_32x32x16_bf16 v[0:15], v[124:127], v[96:99], v[0:15]\n\t" \
    "v_exp_f32 v75, v75\n\t" \
    "v_exp_f32 v76, v76\n\t" \
    "v_cvt_pk_bf16_f32 v101, v74, v75\n\t" \
    "v_exp_f32 v77, v77\n\t" \
    "v_exp_f32 v78, v78\n\t" \
    "v_exp_f32 v79, v79\n\t" \
    "v_cvt_pk_bf16_f32 v102, v76, v77\n\t" \
    "v_cvt_pk_bf16_f32 v103, v78, v79\n\t" \
    "v_and_b32 v100, v100, v148\n\t" \
    "v_and_b32 v101, v101, v149\n\t" \
    "v_and_b32 v102, v102, v150\n\t" \
    "v_and_b32 v103, v103, v151\n\t" \
    "v_mfma_f32_32x32x16_bf16 v[16:31], v[128:131], v[108:111], v[16:31]\n\t" \
    "v_exp_f32 v80, v80\n\t" \
    "v_exp_f32 v81, v81\n\t" \
    "v_exp_f32 v82, v82\n\t" \
    "v_cvt_pk_bf16_f32 v104, v80, v81\n\t" \
    "v_exp_f32 v83, v83\n\t" \
    "v_exp_f32 v84, v84\n\t" \
    "v_cvt_pk_bf16_f32 v105, v82, v83\n\t" \
    "v_exp_f32 v85, v85\n\t" \
    "v_mfma_f32_32x32x16_bf16 v[64:79], v[132:135], v[112:115], v[32:47]\n\t" \
    "v_exp_f32 v86, v86\n\t" \
    "v_cvt_pk_bf16_f32 v106, v84, v85\n\t" \
    "v_exp_f32 v87, v87\n\t" \
    "v_exp_f32 v88, v88\n\t" \
    "v_cvt_pk_bf16_f32 v107, v86, v87\n\t" \
    "v_exp_f32 v89, v89\n\t" \
    "v_exp_f32 v90, v90\n\t" \
    "v_cvt_pk_bf16_f32 v108, v88, v89\n\t" \
    "v_and_b32 v104, v104, v144\n\t" \
    "v_and_b32 v105, v105, v145\n\t" \
    "v_and_b32 v106, v106, v146\n\t" \
    "v_and_b32 v107, v107, v147\n\t" \
    "s_waitcnt lgkmcnt(2)\n\t" \
    "s_nop 0\n\t" \
    "v_mfma_f32_32x32x16_bf16 v[16:31], v[136:139], v[104:107], v[16:31]\n\t" \
    "v_exp_f32 v91, v91\n\t" \
    "v_exp_f32 v92, v92\n\t" \
    "v_cvt_pk_bf16_f32 v109, v90, v91\n\t" \
    "v_exp_f32 v93, v93\n\t" \
    "v_exp_f32 v94, v94\n\t" \
    "v_exp_f32 v95, v95\n\t" \
    "v_cvt_pk_bf16_f32 v110, v92, v93\n\t" \
    "v_cvt_pk_bf16_f32 v111, v94, v95\n\t" \
    "v_and_b32 v108, v108, v148\n\t" \
    "v_and_b32 v109, v109, v149\n\t" \
    "v_and_b32 v110, v110, v150\n\t" \
    "v_and_b32 v111, v111, v151\n\t" \
    "v_mfma_f32_32x32x16_bf16 v[0:15], v[128:131], v[100:103], v[0:15]\n\t" \
    "s_mov_b64 exec, %[vmask]\n\t" \
    "ds_read_b128 v[124:127], v161 offset:4096\n\t" \
    "ds_read_b128 v[128:131], v161 offset:4608\n\t" \
    "s_mov_b64 exec, -1\n\t" \
    "ds_read_b128 v[132:135], v160 offset:6144\n\t" \
    "v_exp_f32 v64, v64\n\t" \
    "v_exp_f32 v65, v65\n\t" \
    "v_exp_f32 v66, v66\n\t" \
    "v_cvt_pk_bf16_f32 v96, v64, v65\n\t" \
    "v_exp_f32 v67, v67\n\t" \
    "v_exp_f32 v68, v68\n\t" \
    "v_cvt_pk_bf16_f32 v97, v66, v67\n\t" \
    "v_exp_f32 v69, v69\n\t" \
    "s_waitcnt lgkmcnt(3)\n\t" \
    "v_mfma_f32_32x32x16_bf16 v[80:95], v[120:123], v[116:119], v[48:63]\n\t" \
    "v_exp_f32 v70, v70\n\t" \
    "v_cvt_pk_bf16_f32 v98, v68, v69\n\t" \
    "v_exp_f32 v71, v71\n\t" \
    "v_exp_f32 v72, v72\n\t" \
    "v_cvt_pk_bf16_f32 v99, v70, v71\n\t" \
    "v_exp_f32 v73, v73\n\t" \
    "v_exp_f32 v74, v74\n\t" \
    "v_cvt_pk_bf16_f32 v100, v72, v73\n\t" \
    "v_mfma_f32_32x32x16_bf16 v[0:15], v[136:139], v[96:99], v[0:15]\n\t" \
    "v_exp_f32 v75, v75\n\t" \
    "v_exp_f32 v76, v76\n\t" \
    "v_cvt_pk_bf16_f32 v101, v74, v75\n\t" \
    "v_exp_f32 v77, v77\n\t" \
    "v_exp_f32 v78, v78\n\t" \
    "v_exp_f32 v79, v79\n\t" \
    "v_cvt_pk_bf16_f32 v102, v76, v77\n\t" \
    "v_cvt_pk_bf16_f32 v103, v78, v79\n\t" \
    "v_mfma_f32_32x32x16_bf16 v[16:31], v[140:143], v[108:111], v[16:31]\n\t" \
    "v_exp_f32 v80, v80\n\t" \
    "v_exp_f32 v81, v81\n\t" \
    "v_exp_f32 v82, v82\n\t" \
    "v_cvt_pk_bf16_f32 v104, v80, v81\n\t" \
    "v_exp_f32 v83, v83\n\t" \
    "v_exp_f32 v84, v84\n\t" \
    "v_cvt_pk_bf16_f32 v105, v82, v83\n\t" \
    "v_exp_f32 v85, v85\n\t" \
    "v_mfma_f32_32x32x16_bf16 v[64:79], v[120:123], v[112:115], v[32:47]\n\t" \
    "v_exp_f32 v86, v86\n\t" \
    "v_cvt_pk_bf16_f32 v106, v84, v85\n\t" \
    "v_exp_f32 v87, v87\n\t" \
    "v_exp_f32 v88, v88\n\t" \
    "v_cvt_pk_bf16_f32 v107, v86, v87\n\t" \
    "v_exp_f32 v89, v89\n\t" \
    "v_exp_f32 v90, v90\n\t" \
    "v_cvt_pk_bf16_f32 v108, v88, v89\n\t" \
    "s_waitcnt lgkmcnt(2)\n\t" \
    "v_mfma_f32_32x32x16_bf16 v[16:31], v[124:127], v[104:107], v[16:31]\n\t" \
    "v_exp_f32 v91, v91\n\t" \
    "v_exp_f32 v92, v92\n\t" \
    "v_cvt_pk_bf16_f32 v109, v90, v91\n\t" \
    "v_exp_f32 v93, v93\n\t" \
    "v_exp_f32 v94, v94\n\t" \
    "v_exp_f32 v95, v95\n\t" \
    "v_cvt_pk_bf16_f32 v110, v92, v93\n\t" \
    "v_cvt_pk_bf16_f32 v111, v94, v95\n\t" \
    "v_mfma_f32_32x32x16_bf16 v[0:15], v[140:143], v[100:103], v[0:15]\n\t" \
    "s_mov_b64 exec, %[vmask]\n\t" \
    "ds_read_b128 v[136:139], v161 offset:6144\n\t" \
    "ds_read_b128 v[140:143], v161 offset:6656\n\t" \
    "s_mov_b64 exec, -1\n\t" \
    "ds_read_b128 v[120:123], v160 offset:8192\n\t" \
    "v_exp_f32 v64, v64\n\t" \
    "v_exp_f32 v65, v65\n\t" \
    "v_exp_f32 v66, v66\n\t" \
    "v_cvt_pk_bf16_f32 v96, v64, v65\n\t" \
    "v_exp_f32 v67, v67\n\t" \
    "v_exp_f32 v68, v68\n\t" \
    "v_cvt_pk_bf16_f32 v97, v66, v67\n\t" \
    "v_exp_f32 v69, v69\n\t" \
    "s_waitcnt lgkmcnt(3)\n\t" \
    "v_mfma_f32_32x32x16_bf16 v[80:95], v[132:135], v[116:119], v[48:63]\n\t" \
    "v_exp_f32 v70, v70\n\t" \
    "v_cvt_pk_bf16_f32 v98, v68, v69\n\t" \
    "v_exp_f32 v71, v71\n\t" \
    "v_exp_f32 v72, v72\n\t" \
    "v_cvt_pk_bf16_f32 v99, v70, v71\n\t" \
    "v_exp_f32 v73, v73\n\t" \
    "v_exp_f32 v74, v74\n\t" \
    "v_cvt_pk_bf16_f32 v100, v72, v73\n\t" \
    "v_mfma_f32_32x32x16_bf16 v[0:15], v[124:127], v[96:99], v[0:15]\n\t" \
    "v_exp_f32 v75, v75\n\t" \
    "v_exp_f32 v76, v76\n\t" \
    "v_cvt_pk_bf16_f32 v101, v74, v75\n\t" \
    "v_exp_f32 v77, v77\n\t" \
    "v_exp_f32 v78, v78\n\t" \
    "v_exp_f32 v79, v79\n\t" \
    "v_cvt_pk_bf16_f32 v102, v76, v77\n\t" \
    "v_cvt_pk_bf16_f32 v103, v78, v79\n\t" \
    "v_mfma_f32_32x32x16_bf16 v[16:31], v[128:131], v[108:111], v[16:31]\n\t" \
    "v_exp_f32 v80, v80\n\t" \
    "v_exp_f32 v81, v81\n\t" \
    "v_exp_f32 v82, v82\n\t" \
    "v_cvt_pk_bf16_f32 v104, v80, v81\n\t" \
    "v_exp_f32 v83, v83\n\t" \
    "v_exp_f32 v84, v84\n\t" \
    "v_cvt_pk_bf16_f32 v105, v82, v83\n\t" \
    "v_exp_f32 v85, v85\n\t" \
    "v_mfma_f32_32x32x16_bf16 v[64:79], v[132:135], v[112:115], v[32:47]\n\t" \
    "v_exp_f32 v86, v86\n\t" \
    "v_cvt_pk_bf16_f32 v106, v84, v85\n\t" \
    "v_exp_f32 v87, v87\n\t" \
    "v_exp_f32 v88, v88\n\t" \
    "v_cvt_pk_bf16_f32 v107, v86, v87\n\t" \
    "v_exp_f32 v89, v89\n\t" \
    "v_exp_f32 v90, v90\n\t" \
    "v_cvt_pk_bf16_f32 v108, v88, v89\n\t" \
    "s_waitcnt lgkmcnt(2)\n\t" \
    "v_mfma_f32_32x32x16_bf16 v[16:31], v[136:139], v[104:107], v[16:31]\n\t" \
    "v_exp_f32 v91, v91\n\t" \
    "v_exp_f32 v92, v92\n\t" \
    "v_cvt_pk_bf16_f32 v109, v90, v91\n\t" \
    "v_exp_f32 v93, v93\n\t" \
    "v_exp_f32 v94, v94\n\t" \
    "v_exp_f32 v95, v95\n\t" \
    "v_cvt_pk_bf16_f32 v110, v92, v93\n\t" \
    "v_cvt_pk_bf16_f32 v111, v94, v95\n\t" \
    "v_mfma_f32_32x32x16_bf16 v[0:15], v[128:131], v[100:103], v[0:15]\n\t" \
    "s_mov_b64 exec, %[vmask]\n\t" \
    "ds_read_b128 v[124:127], v161 offset:8192\n\t" \
    "ds_read_b128 v[128:131], v161 offset:8704\n\t" \
    "s_mov_b64 exec, -1\n\t" \
    "ds_read_b128 v[132:135], v160 offset:10240\n\t" \
    "v_exp_f32 v64, v64\n\t" \
    "v_exp_f32 v65, v65\n\t" \
    "v_exp_f32 v66, v66\n\t" \
    "v_cvt_pk_bf16_f32 v96, v64, v65\n\t" \
    "v_exp_f32 v67, v67\n\t" \
    "v_exp_f32 v68, v68\n\t" \
    "v_cvt_pk_bf16_f32 v97, v66, v67\n\t" \
    "v_exp_f32 v69, v69\n\t" \
    "s_waitcnt lgkmcnt(3)\n\t" \
    "v_mfma_f32_32x32x16_bf16 v[80:95], v[120:123], v[116:119], v[48:63]\n\t" \
    "v_exp_f32 v70, v70\n\t" \
    "v_cvt_pk_bf16_f32 v98, v68, v69\n\t" \
    "v_exp_f32 v71, v71\n\t" \
    "v_exp_f32 v72, v72\n\t" \
    "v_cvt_pk_bf16_f32 v99, v70, v71\n\t" \
    "v_exp_f32 v73, v73\n\t" \
    "v_exp_f32 v74, v74\n\t" \
    "v_cvt_pk_bf16_f32 v100, v72, v73\n\t" \
    "v_mfma_f32_32x32x16_bf16 v[0:15], v[136:139], v[96:99], v[0:15]\n\t" \
    "v_exp_f32 v75, v75\n\t" \
    "v_exp_f32 v76, v76\n\t" \
    "v_cvt_pk_bf16_f32 v101, v74, v75\n\t" \
    "v_exp_f32 v77, v77\n\t" \
    "v_exp_f32 v78, v78\n\t" \
    "v_exp_f32 v79, v79\n\t" \
    "v_cvt_pk_bf16_f32 v102, v76, v77\n\t" \
    "v_cvt_pk_bf16_f32 v103, v78, v79\n\t" \
    "v_mfma_f32_32x32x16_bf16 v[16:31], v[140:143], v[108:111], v[16:31]\n\t" \
    "v_exp_f32 v80, v80\n\t" \
    "v_exp_f32 v81, v81\n\t" \
    "v_exp_f32 v82, v82\n\t" \
    "v_cvt_pk_bf16_f32 v104, v80, v81\n\t" \
    "v_exp_f32 v83, v83\n\t" \
    "v_exp_f32 v84, v84\n\t" \
    "v_cvt_pk_bf16_f32 v105, v82, v83\n\t" \
    "v_exp_f32 v85, v85\n\t" \
    "v_mfma_f32_32x32x16_bf16 v[64:79], v[120:123], v[112:115], v[32:47]\n\t" \
    "v_exp_f32 v86, v86\n\t" \
    "v_cvt_pk_bf16_f32 v106, v84, v85\n\t" \
    "v_exp_f32 v87, v87\n\t" \
    "v_exp_f32 v88, v88\n\t" \
    "v_cvt_pk_bf16_f32 v107, v86, v87\n\t" \
    "v_exp_f32 v89, v89\n\t" \
    "v_exp_f32 v90, v90\n\t" \
    "v_cvt_pk_bf16_f32 v108, v88, v89\n\t" \
    "s_waitcnt lgkmcnt(2)\n\t" \
    "v_mfma_f32_32x32x16_bf16 v[16:31], v[124:127], v[104:107], v[16:31]\n\t" \
    "v_exp_f32 v91, v91\n\t" \
    "v_exp_f32 v92, v92\n\t" \
    "v_cvt_pk_bf16_f32 v109, v90, v91\n\t" \
    "v_exp_f32 v93, v93\n\t" \
    "v_exp_f32 v94, v94\n\t" \
    "v_exp_f32 v95, v95\n\t" \
    "v_cvt_pk_bf16_f32 v110, v92, v93\n\t" \
    "v_cvt_pk_bf16_f32 v111, v94, v95\n\t" \
    "v_mfma_f32_32x32x16_bf16 v[0:15], v[140:143], v[100:103], v[0:15]\n\t" \
    "s_mov_b64 exec, %[vmask]\n\t" \
    "ds_read_b128 v[136:139], v161 offset:10240\n\t" \
    "ds_read_b128 v[140:143], v161 offset:10752\n\t" \
    "s_mov_b64 exec, -1\n\t" \
    "ds_read_b128 v[120:123], v160 offset:12288\n\t" \
    "v_exp_f32 v64, v64\n\t" \
    "v_exp_f32 v65, v65\n\t" \
    "v_exp_f32 v66, v66\n\t" \
    "v_cvt_pk_bf16_f32 v96, v64, v65\n\t" \
    "v_exp_f32 v67, v67\n\t" \
    "v_exp_f32 v68, v68\n\t" \
    "v_cvt_pk_bf16_f32 v97, v66, v67\n\t" \
    "v_exp_f32 v69, v69\n\t" \
    "s_waitcnt lgkmcnt(3)\n\t" \
    "v_mfma_f32_32x32x16_bf16 v[80:95], v[132:135], v[116:119], v[48:63]\n\t" \
    "v_exp_f32 v70, v70\n\t" \
    "v_cvt_pk_bf16_f32 v98, v68, v69\n\t" \
    "v_exp_f32 v71, v71\n\t" \
    "v_exp_f32 v72, v72\n\t" \
    "v_cvt_pk_bf16_f32 v99, v70, v71\n\t" \
    "v_exp_f32 v73, v73\n\t" \
    "v_exp_f32 v74, v74\n\t" \
    "v_cvt_pk_bf16_f32 v100, v72, v73\n\t" \
    "v_mfma_f32_32x32x16_bf16 v[0:15], v[124:127], v[96:99], v[0:15]\n\t" \
    "v_exp_f32 v75, v75\n\t" \
    "v_exp_f32 v76, v76\n\t" \
    "v_cvt_pk_bf16_f32 v101, v74, v75\n\t" \
    "v_exp_f32 v77, v77\n\t" \
    "v_exp_f32 v78, v78\n\t" \
    "v_exp_f32 v79, v79\n\t" \
    "v_cvt_pk_bf16_f32 v102, v76, v77\n\t" \
    "v_cvt_pk_bf16_f32 v103, v78, v79\n\t" \
    "v_mfma_f32_32x32x16_bf16 v[16:31], v[128:131], v[108:111], v[16:31]\n\t" \
    "v_exp_f32 v80, v80\n\t" \
    "v_exp_f32 v81, v81\n\t" \
    "v_exp_f32 v82, v82\n\t" \
    "v_cvt_pk_bf16_f32 v104, v80, v81\n\t" \
    "v_exp_f32 v83, v83\n\t" \
    "v_exp_f32 v84, v84\n\t" \
    "v_cvt_pk_bf16_f32 v105, v82, v83\n\t" \
    "v_exp_f32 v85, v85\n\t" \
    "v_mfma_f32_32x32x16_bf16 v[64:79], v[132:135], v[112:115], v[32:47]\n\t" \
    "v_exp_f32 v86, v86\n\t" \
    "v_cvt_pk_bf16_f32 v106, v84, v85\n\t" \
    "v_exp_f32 v87, v87\n\t" \
    "v_exp_f32 v88, v88\n\t" \
    "v_cvt_pk_bf16_f32 v107, v86, v87\n\t" \
    "v_exp_f32 v89, v89\n\t" \
    "v_exp_f32 v90, v90\n\t" \
    "v_cvt_pk_bf16_f32 v108, v88, v89\n\t" \
    "s_waitcnt lgkmcnt(2)\n\t" \
    "v_mfma_f32_32x32x16_bf16 v[16:31], v[136:139], v[104:107], v[16:31]\n\t" \
    "v_exp_f32 v91, v91\n\t" \
    "v_exp_f32 v92, v92\n\t" \
    "v_cvt_pk_bf16_f32 v109, v90, v91\n\t" \
    "v_exp_f32 v93, v93\n\t" \
    "v_exp_f32 v94, v94\n\t" \
    "v_exp_f32 v95, v95\n\t" \
    "v_cvt_pk_bf16_f32 v110, v92, v93\n\t" \
    "v_cvt_pk_bf16_f32 v111, v94, v95\n\t" \
    "v_mfma_f32_32x32x16_bf16 v[0:15], v[128:131], v[100:103], v[0:15]\n\t" \
    "s_mov_b64 exec, %[vmask]\n\t" \
    "ds_read_b128 v[124:127], v161 offset:12288\n\t" \
    "ds_read_b128 v[128:131], v161 offset:12800\n\t" \
    "s_mov_b64 exec, -1\n\t" \
    "ds_read_b128 v[132:135], v160 offset:14336\n\t" \
    "v_exp_f32 v64, v64\n\t" \
    "v_exp_f32 v65, v65\n\t" \
    "v_exp_f32 v66, v66\n\t" \
    "v_cvt_pk_bf16_f32 v96, v64, v65\n\t" \
    "v_exp_f32 v67, v67\n\t" \
    "v_exp_f32 v68, v68\n\t" \
    "v_cvt_pk_bf16_f32 v97, v66, v67\n\t" \
    "v_exp_f32 v69, v69\n\t" \
    "s_waitcnt lgkmcnt(3)\n\t" \
    "v_mfma_f32_32x32x16_bf16 v[80:95], v[120:123], v[116:119], v[48:63]\n\t" \
    "v_exp_f32 v70, v70\n\t" \
    "v_cvt_pk_bf16_f32 v98, v68, v69\n\t" \
    "v_exp_f32 v71, v71\n\t" \
    "v_exp_f32 v72, v72\n\t" \
    "v_cvt_pk_bf16_f32 v99, v70, v71\n\t" \
    "v_exp_f32 v73, v73\n\t" \
    "v_exp_f32 v74, v74\n\t" \
    "v_cvt_pk_bf16_f32 v100, v72, v73\n\t" \
    "v_mfma_f32_32x32x16_bf16 v[0:15], v[136:139], v[96:99], v[0:15]\n\t" \
    "v_exp_f32 v75, v75\n\t" \
    "v_exp_f32 v76, v76\n\t" \
    "v_cvt_pk_bf16_f32 v101, v74, v75\n\t" \
    "v_exp_f32 v77, v77\n\t" \
    "v_exp_f32 v78, v78\n\t" \
    "v_exp_f32 v79, v79\n\t" \
    "v_cvt_pk_bf16_f32 v102, v76, v77\n\t" \
    "v_cvt_pk_bf16_f32 v103, v78, v79\n\t" \
    "v_mfma_f32_32x32x16_bf16 v[16:31], v[140:143], v[108:111], v[16:31]\n\t" \
    "v_exp_f32 v80, v80\n\t" \
    "v_exp_f32 v81, v81\n\t" \
    "v_exp_f32 v82, v82\n\t" \
    "v_cvt_pk_bf16_f32 v104, v80, v81\n\t" \
    "v_exp_f32 v83, v83\n\t" \
    "v_exp_f32 v84, v84\n\t" \
    "v_cvt_pk_bf16_f32 v105, v82, v83\n\t" \
    "v_exp_f32 v85, v85\n\t" \
    "v_mfma_f32_32x32x16_bf16 v[64:79], v[120:123], v[112:115], v[32:47]\n\t" \
    "v_exp_f32 v86, v86\n\t" \
    "v_cvt_pk_bf16_f32 v106, v84, v85\n\t" \
    "v_exp_f32 v87, v87\n\t" \
    "v_exp_f32 v88, v88\n\t" \
    "v_cvt_pk_bf16_f32 v107, v86, v87\n\t" \
    "v_exp_f32 v89, v89\n\t" \
    "v_exp_f32 v90, v90\n\t" \
    "v_cvt_pk_bf16_f32 v108, v88, v89\n\t" \
    "s_waitcnt lgkmcnt(2)\n\t" \
    "v_mfma_f32_32x32x16_bf16 v[16:31], v[124:127], v[104:107], v[16:31]\n\t" \
    "v_exp_f32 v91, v91\n\t" \
    "v_exp_f32 v92, v92\n\t" \
    "v_cvt_pk_bf16_f32 v109, v90, v91\n\t" \
    "v_exp_f32 v93, v93\n\t" \
    "v_exp_f32 v94, v94\n\t" \
    "v_exp_f32 v95, v95\n\t" \
    "v_cvt_pk_bf16_f32 v110, v92, v93\n\t" \
    "v_cvt_pk_bf16_f32 v111, v94, v95\n\t" \
    "v_mfma_f32_32x32x16_bf16 v[0:15], v[140:143], v[100:103], v[0:15]\n\t" \
    "s_mov_b64 exec, %[vmask]\n\t" \
    "ds_read_b128 v[136:139], v161 offset:14336\n\t" \
    "ds_read_b128 v[140:143], v161 offset:14848\n\t" \
    "s_mov_b64 exec, -1\n\t" \
    "ds_read_b128 v[120:123], v160 offset:16384\n\t" \
    "v_exp_f32 v64, v64\n\t" \
    "v_exp_f32 v65, v65\n\t" \
    "v_exp_f32 v66, v66\n\t" \
    "v_cvt_pk_bf16_f32 v96, v64, v65\n\t" \
    "v_exp_f32 v67, v67\n\t" \
    "v_exp_f32 v68, v68\n\t" \
    "v_cvt_pk_bf16_f32 v97, v66, v67\n\t" \
    "v_exp_f32 v69, v69\n\t" \
    "s_waitcnt lgkmcnt(3)\n\t" \
    "v_mfma_f32_32x32x16_bf16 v[80:95], v[132:135], v[116:119], v[48:63]\n\t" \
    "v_exp_f32 v70, v70\n\t" \
    "v_cvt_pk_bf16_f32 v98, v68, v69\n\t" \
    "v_exp_f32 v71, v71\n\t" \
    "v_exp_f32 v72, v72\n\t" \
    "v_cvt_pk_bf16_f32 v99, v70, v71\n\t" \
    "v_exp_f32 v73, v73\n\t" \
    "v_exp_f32 v74, v74\n\t" \
    "v_cvt_pk_bf16_f32 v100, v72, v73\n\t" \
    "v_mfma_f32_32x32x16_bf16 v[0:15], v[124:127], v[96:99], v[0:15]\n\t" \
    "v_exp_f32 v75, v75\n\t" \
    "v_exp_f32 v76, v76\n\t" \
    "v_cvt_pk_bf16_f32 v101, v74, v75\n\t" \
    "v_exp_f32 v77, v77\n\t" \
    "v_exp_f32 v78, v78\n\t" \
    "v_exp_f32 v79, v79\n\t" \
    "v_cvt_pk_bf16_f32 v102, v76, v77\n\t" \
    "v_cvt_pk_bf16_f32 v103, v78, v79\n\t" \
    "v_mfma_f32_32x32x16_bf16 v[16:31], v[128:131], v[108:111], v[16:31]\n\t" \
    "v_exp_f32 v80, v80\n\t" \
    "v_exp_f32 v81, v81\n\t" \
    "v_exp_f32 v82, v82\n\t" \
    "v_cvt_pk_bf16_f32 v104, v80, v81\n\t" \
    "v_exp_f32 v83, v83\n\t" \
    "v_exp_f32 v84, v84\n\t" \
    "v_cvt_pk_bf16_f32 v105, v82, v83\n\t" \
    "v_exp_f32 v85, v85\n\t" \
    "v_mfma_f32_32x32x16_bf16 v[64:79], v[132:135], v[112:115], v[32:47]\n\t" \
    "v_exp_f32 v86, v86\n\t" \
    "v_cvt_pk_bf16_f32 v106, v84, v85\n\t" \
    "v_exp_f32 v87, v87\n\t" \
    "v_exp_f32 v88, v88\n\t" \
    "v_cvt_pk_bf16_f32 v107, v86, v87\n\t" \
    "v_exp_f32 v89, v89\n\t" \
    "v_exp_f32 v90, v90\n\t" \
    "v_cvt_pk_bf16_f32 v108, v88, v89\n\t" \
    "s_waitcnt lgkmcnt(2)\n\t" \
    "v_mfma_f32_32x32x16_bf16 v[16:31], v[136:139], v[104:107], v[16:31]\n\t" \
    "v_exp_f32 v91, v91\n\t" \
    "v_exp_f32 v92, v92\n\t" \
    "v_cvt_pk_bf16_f32 v109, v90, v91\n\t" \
    "v_exp_f32 v93, v93\n\t" \
    "v_exp_f32 v94, v94\n\t" \
    "v_exp_f32 v95, v95\n\t" \
    "v_cvt_pk_bf16_f32 v110, v92, v93\n\t" \
    "v_cvt_pk_bf16_f32 v111, v94, v95\n\t" \
    "v_mfma_f32_32x32x16_bf16 v[0:15], v[128:131], v[100:103], v[0:15]\n\t" \
    "s_mov_b64 exec, %[vmask]\n\t" \
    "ds_read_b128 v[124:127], v161 offset:16384\n\t" \
    "ds_read_b128 v[128:131], v161 offset:16896\n\t" \
    "s_mov_b64 exec, -1\n\t" \
    "ds_read_b128 v[132:135], v160 offset:18432\n\t" \
    "v_exp_f32 v64, v64\n\t" \
    "v_exp_f32 v65, v65\n\t" \
    "v_exp_f32 v66, v66\n\t" \
    "v_cvt_pk_bf16_f32 v96, v64, v65\n\t" \
    "v_exp_f32 v67, v67\n\t" \
    "v_exp_f32 v68, v68\n\t" \
    "v_cvt_pk_bf16_f32 v97, v66, v67\n\t" \
    "v_exp_f32 v69, v69\n\t" \
    "s_waitcnt lgkmcnt(3)\n\t" \
    "v_mfma_f32_32x32x16_bf16 v[80:95], v[120:123], v[116:119], v[48:63]\n\t" \
    "v_exp_f32 v70, v70\n\t" \
    "v_cvt_pk_bf16_f32 v98, v68, v69\n\t" \
    "v_exp_f32 v71, v71\n\t" \
    "v_exp_f32 v72, v72\n\t" \
    "v_cvt_pk_bf16_f32 v99, v70, v71\n\t" \
    "v_exp_f32 v73, v73\n\t" \
    "v_exp_f32 v74, v74\n\t" \
    "v_cvt_pk_bf16_f32 v100, v72, v73\n\t" \
    "v_mfma_f32_32x32x16_bf16 v[0:15], v[136:139], v[96:99], v[0:15]\n\t" \
    "v_exp_f32 v75, v75\n\t" \
    "v_exp_f32 v76, v76\n\t" \
    "v_cvt_pk_bf16_f32 v101, v74, v75\n\t" \
    "v_exp_f32 v77, v77\n\t" \
    "v_exp_f32 v78, v78\n\t" \
    "v_exp_f32 v79, v79\n\t" \
    "v_cvt_pk_bf16_f32 v102, v76, v77\n\t" \
    "v_cvt_pk_bf16_f32 v103, v78, v79\n\t" \
    "v_mfma_f32_32x32x16_bf16 v[16:31], v[140:143], v[108:111], v[16:31]\n\t" \
    "v_exp_f32 v80, v80\n\t" \
    "v_exp_f32 v81, v81\n\t" \
    "v_exp_f32 v82, v82\n\t" \
    "v_cvt_pk_bf16_f32 v104, v80, v81\n\t" \
    "v_exp_f32 v83, v83\n\t" \
    "v_exp_f32 v84, v84\n\t" \
    "v_cvt_pk_bf16_f32 v105, v82, v83\n\t" \
    "v_exp_f32 v85, v85\n\t" \
    "v_mfma_f32_32x32x16_bf16 v[64:79], v[120:123], v[112:115], v[32:47]\n\t" \
    "v_exp_f32 v86, v86\n\t" \
    "v_cvt_pk_bf16_f32 v106, v84, v85\n\t" \
    "v_exp_f32 v87, v87\n\t" \
    "v_exp_f32 v88, v88\n\t" \
    "v_cvt_pk_bf16_f32 v107, v86, v87\n\t" \
    "v_exp_f32 v89, v89\n\t" \
    "v_exp_f32 v90, v90\n\t" \
    "v_cvt_pk_bf16_f32 v108, v88, v89\n\t" \
    "s_waitcnt lgkmcnt(2)\n\t" \
    "v_mfma_f32_32x32x16_bf16 v[16:31], v[124:127], v[104:107], v[16:31]\n\t" \
    "v_exp_f32 v91, v91\n\t" \
    "v_exp_f32 v92, v92\n\t" \
    "v_cvt_pk_bf16_f32 v109, v90, v91\n\t" \
    "v_exp_f32 v93, v93\n\t" \
    "v_exp_f32 v94, v94\n\t" \
    "v_exp_f32 v95, v95\n\t" \
    "v_cvt_pk_bf16_f32 v110, v92, v93\n\t" \
    "v_cvt_pk_bf16_f32 v111, v94, v95\n\t" \
    "v_mfma_f32_32x32x16_bf16 v[0:15], v[140:143], v[100:103], v[0:15]\n\t" \
    "s_mov_b64 exec, %[vmask]\n\t" \
    "ds_read_b128 v[136:139], v161 offset:18432\n\t" \
    "ds_read_b128 v[140:143], v161 offset:18944\n\t" \
    "s_mov_b64 exec, -1\n\t" \
    "v_exp_f32 v64, v64\n\t" \
    "v_exp_f32 v65, v65\n\t" \
    "v_exp_f32 v66, v66\n\t" \
    "v_cvt_pk_bf16_f32 v96, v64, v65\n\t" \
    "v_exp_f32 v67, v67\n\t" \
    "v_exp_f32 v68, v68\n\t" \
    "v_cvt_pk_bf16_f32 v97, v66, v67\n\t" \
    "v_exp_f32 v69, v69\n\t" \
    "s_waitcnt lgkmcnt(2)\n\t" \
    "v_mfma_f32_32x32x16_bf16 v[80:95], v[132:135], v[116:119], v[48:63]\n\t" \
    "v_exp_f32 v70, v70\n\t" \
    "v_cvt_pk_bf16_f32 v98, v68, v69\n\t" \
    "v_exp_f32 v71, v71\n\t" \
    "v_exp_f32 v72, v72\n\t" \
    "v_cvt_pk_bf16_f32 v99, v70, v71\n\t" \
    "v_exp_f32 v73, v73\n\t" \
    "v_exp_f32 v74, v74\n\t" \
    "v_cvt_pk_bf16_f32 v100, v72, v73\n\t" \
    "v_and_b32 v96, v96, v152\n\t" \
    "v_and_b32 v97, v97, v153\n\t" \
    "v_and_b32 v98, v98, v154\n\t" \
    "v_and_b32 v99, v99, v155\n\t" \
    "s_nop 1\n\t" \
    "v_mfma_f32_32x32x16_bf16 v[0:15], v[124:127], v[96:99], v[0:15]\n\t" \
    "v_exp_f32 v75, v75\n\t" \
    "v_exp_f32 v76, v76\n\t" \
    "v_cvt_pk_bf16_f32 v101, v74, v75\n\t" \
    "v_exp_f32 v77, v77\n\t" \
    "v_exp_f32 v78, v78\n\t" \
    "v_exp_f32 v79, v79\n\t" \
    "v_cvt_pk_bf16_f32 v102, v76, v77\n\t" \
    "v_cvt_pk_bf16_f32 v103, v78, v79\n\t" \
    "v_and_b32 v100, v100, v156\n\t" \
    "v_and_b32 v101, v101, v157\n\t" \
    "v_and_b32 v102, v102, v158\n\t" \
    "v_and_b32 v103, v103, v159\n\t" \
    "v_mfma_f32_32x32x16_bf16 v[16:31], v[128:131], v[108:111], v[16:31]\n\t" \
    "v_exp_f32 v80, v80\n\t" \
    "v_exp_f32 v81, v81\n\t" \
    "v_exp_f32 v82, v82\n\t" \
    "v_cvt_pk_bf16_f32 v104, v80, v81\n\t" \
    "v_exp_f32 v83, v83\n\t" \
    "v_exp_f32 v84, v84\n\t" \
    "v_cvt_pk_bf16_f32 v105, v82, v83\n\t" \
    "v_exp_f32 v85, v85\n\t" \
    "v_exp_f32 v86, v86\n\t" \
    "v_cvt_pk_bf16_f32 v106, v84, v85\n\t" \
    "v_exp_f32 v87, v87\n\t" \
    "v_exp_f32 v88, v88\n\t" \
    "v_cvt_pk_bf16_f32 v107, v86, v87\n\t" \
    "v_exp_f32 v89, v89\n\t" \
    "v_exp_f32 v90, v90\n\t" \
    "v_cvt_pk_bf16_f32 v108, v88, v89\n\t" \
    "v_and_b32 v104, v104, v152\n\t" \
    "v_and_b32 v105, v105, v153\n\t" \
    "v_and_b32 v106, v106, v154\n\t" \
    "v_and_b32 v107, v107, v155\n\t" \
    "s_waitcnt lgkmcnt(1)\n\t" \
    "s_nop 0\n\t" \
    "v_mfma_f32_32x32x16_bf16 v[16:31], v[136:139], v[104:107], v[16:31]\n\t" \
    "v_exp_f32 v91, v91\n\t" \
    "v_exp_f32 v92, v92\n\t" \
    "v_cvt_pk_bf16_f32 v109, v90, v91\n\t" \
    "v_exp_f32 v93, v93\n\t" \
    "v_exp_f32 v94, v94\n\t" \
    "v_exp_f32 v95, v95\n\t" \
    "v_cvt_pk_bf16_f32 v110, v92, v93\n\t" \
    "v_cvt_pk_bf16_f32 v111, v94, v95\n\t" \
    "v_and_b32 v108, v108, v156\n\t" \
    "v_and_b32 v109, v109, v157\n\t" \
    "v_and_b32 v110, v110, v158\n\t" \
    "v_and_b32 v111, v111, v159\n\t" \
    "v_mfma_f32_32x32x16_bf16 v[0:15], v[128:131], v[100:103], v[0:15]\n\t" \
    "s_waitcnt lgkmcnt(0)\n\t" \
    "s_nop 1\n\t" \
    "v_mfma_f32_32x32x16_bf16 v[16:31], v[140:143], v[108:111], v[16:31]\n\t" \
    "s_nop 15\n\t"

#define ATTN_BH_ASM \
    "v_mov_b32 v124, v162\n\t" \
    "v_mov_b32 v125, v162\n\t" \
    "v_mov_b32 v126, v162\n\t" \
    "v_mov_b32 v127, v162\n\t" \
    "v_mov_b32 v128, v162\n\t" \
    "v_mov_b32 v129, v162\n\t" \
    "v_mov_b32 v130, v162\n\t" \
    "v_mov_b32 v131, v162\n\t" \
    "v_mov_b32 v136, v162\n\t" \
    "v_mov_b32 v137, v162\n\t" \
    "v_mov_b32 v138, v162\n\t" \
    "v_mov_b32 v139, v162\n\t" \
    "v_mov_b32 v140, v162\n\t" \
    "v_mov_b32 v141, v162\n\t" \
    "v_mov_b32 v142, v162\n\t" \
    "v_mov_b32 v143, v162\n\t" \
    "v_mov_b32 v108, 0\n\t" \
    "v_mov_b32 v109, 0\n\t" \
    "v_mov_b32 v110, 0\n\t" \
    "v_mov_b32 v111, 0\n\t" \
    "ds_read_b128 v[120:123], v160\n\t" \
    "s_mov_b64 exec, %[vmask]\n\t" \
    "ds_read_b128 v[124:127], v161\n\t" \
    "ds_read_b128 v[128:131], v161 offset:512\n\t" \
    "s_mov_b64 exec, -1\n\t" \
    "ds_read_b128 v[132:135], v160 offset:2048\n\t" \
    "s_mov_b64 exec, %[vmask]\n\t" \
    "ds_read_b128 v[136:139], v161 offset:2048\n\t" \
    "ds_read_b128 v[140:143], v161 offset:2560\n\t" \
    "s_mov_b64 exec, -1\n\t" \
    "s_waitcnt lgkmcnt(3)\n\t" \
    "v_mfma_f32_32x32x16_bf16 v[64:79], v[120:123], v[112:115], v[32:47]\n\t" \
    "s_nop 15\n\t" \
    "ds_read_b128 v[120:123], v160 offset:4096\n\t" \
    "v_exp_f32 v64, v64\n\t" \
    "v_exp_f32 v65, v65\n\t" \
    "v_exp_f32 v66, v66\n\t" \
    "v_cvt_pk_bf16_f32 v96, v64, v65\n\t" \
    "v_exp_f32 v67, v67\n\t" \
    "v_exp_f32 v68, v68\n\t" \
    "v_cvt_pk_bf16_f32 v97, v66, v67\n\t" \
    "v_exp_f32 v69, v69\n\t" \
    "s_waitcnt lgkmcnt(3)\n\t" \
    "v_mfma_f32_32x32x16_bf16 v[80:95], v[132:135], v[116:119], v[48:63]\n\t" \
    "v_exp_f32 v70, v70\n\t" \
    "v_cvt_pk_bf16_f32 v98, v68, v69\n\t" \
    "v_exp_f32 v71, v71\n\t" \
    "v_exp_f32 v72, v72\n\t" \
    "v_cvt_pk_bf16_f32 v99, v70, v71\n\t" \
    "v_exp_f32 v73, v73\n\t" \
    "v_exp_f32 v74, v74\n\t" \
    "v_cvt_pk_bf16_f32 v100, v72, v73\n\t" \
    "s_cmp_ge_u32 0, %[tmin]\n\t" \
    "s_cselect_b32 %[sm], -1, 0\n\t" \
    "v_and_b32 v96, %[sm], v96\n\t" \
    "v_and_b32 v97, %[sm], v97\n\t" \
    "v_and_b32 v98, %[sm], v98\n\t" \
    "v_and_b32 v99, %[sm], v99\n\t" \
    "s_nop 1\n\t" \
    "v_mfma_f32_32x32x16_bf16 v[0:15], v[124:127], v[96:99], v[0:15]\n\t" \
    "v_exp_f32 v75, v75\n\t" \
    "v_exp_f32 v76, v76\n\t" \
    "v_cvt_pk_bf16_f32 v101, v74, v75\n\t" \
    "v_exp_f32 v77, v77\n\t" \
    "v_exp_f32 v78, v78\n\t" \
    "v_exp_f32 v79, v79\n\t" \
    "v_cvt_pk_bf16_f32 v102, v76, v77\n\t" \
    "v_cvt_pk_bf16_f32 v103, v78, v79\n\t" \
    "v_and_b32 v100, %[sm], v100\n\t" \
    "v_and_b32 v101, %[sm], v101\n\t" \
    "v_and_b32 v102, %[sm], v102\n\t" \
    "v_and_b32 v103, %[sm], v103\n\t" \
    "v_mfma_f32_32x32x16_bf16 v[16:31], v[128:131], v[108:111], v[16:31]\n\t" \
    "v_exp_f32 v80, v80\n\t" \
    "v_exp_f32 v81, v81\n\t" \
    "v_exp_f32 v82, v82\n\t" \
    "v_cvt_pk_bf16_f32 v104, v80, v81\n\t" \
    "v_exp_f32 v83, v83\n\t" \
    "v_exp_f32 v84, v84\n\t" \
    "v_cvt_pk_bf16_f32 v105, v82, v83\n\t" \
    "v_exp_f32 v85, v85\n\t" \
    "v_mfma_f32_32x32x16_bf16 v[64:79], v[132:135], v[112:115], v[32:47]\n\t" \
    "v_exp_f32 v86, v86\n\t" \
    "v_cvt_pk_bf16_f32 v106, v84, v85\n\t" \
    "v_exp_f32 v87, v87\n\t" \
    "v_exp_f32 v88, v88\n\t" \
    "v_cvt_pk_bf16_f32 v107, v86, v87\n\t" \
    "v_exp_f32 v89, v89\n\t" \
    "v_exp_f32 v90, v90\n\t" \
    "v_cvt_pk_bf16_f32 v108, v88, v89\n\t" \
    "s_cmp_ge_u32 1, %[tmin]\n\t" \
    "s_cselect_b32 %[sm], -1, 0\n\t" \
    "v_and_b32 v104, %[sm], v104\n\t" \
    "v_and_b32 v105, %[sm], v105\n\t" \
    "v_and_b32 v106, %[sm], v106\n\t" \
    "v_and_b32 v107, %[sm], v107\n\t" \
    "s_nop 0\n\t" \
    "s_waitcnt lgkmcnt(2)\n\t" \
    "v_mfma_f32_32x32x16_bf16 v[16:31], v[136:139], v[104:107], v[16:31]\n\t" \
    "v_exp_f32 v91, v91\n\t" \
    "v_exp_f32 v92, v92\n\t" \
    "v_cvt_pk_bf16_f32 v109, v90, v91\n\t" \
    "v_exp_f32 v93, v93\n\t" \
    "v_exp_f32 v94, v94\n\t" \
    "v_exp_f32 v95, v95\n\t" \
    "v_cvt_pk_bf16_f32 v110, v92, v93\n\t" \
    "v_cvt_pk_bf16_f32 v111, v94, v95\n\t" \
    "v_and_b32 v108, %[sm], v108\n\t" \
    "v_and_b32 v109, %[sm], v109\n\t" \
    "v_and_b32 v110, %[sm], v110\n\t" \
    "v_and_b32 v111, %[sm], v111\n\t" \
    "v_mfma_f32_32x32x16_bf16 v[0:15], v[128:131], v[100:103], v[0:15]\n\t" \
    "s_mov_b64 exec, %[vmask]\n\t" \
    "ds_read_b128 v[124:127], v161 offset:4096\n\t" \
    "ds_read_b128 v[128:131], v161 offset:4608\n\t" \
    "s_mov_b64 exec, -1\n\t" \
    "ds_read_b128 v[132:135], v160 offset:6144\n\t" \
    "v_exp_f32 v64, v64\n\t" \
    "v_exp_f32 v65, v65\n\t" \
    "v_exp_f32 v66, v66\n\t" \
    "v_cvt_pk_bf16_f32 v96, v64, v65\n\t" \
    "v_exp_f32 v67, v67\n\t" \
    "v_exp_f32 v68, v68\n\t" \
    "v_cvt_pk_bf16_f32 v97, v66, v67\n\t" \
    "v_exp_f32 v69, v69\n\t" \
    "s_waitcnt lgkmcnt(3)\n\t" \
    "v_mfma_f32_32x32x16_bf16 v[80:95], v[120:123], v[116:119], v[48:63]\n\t" \
    "v_exp_f32 v70, v70\n\t" \
    "v_cvt_pk_bf16_f32 v98, v68, v69\n\t" \
    "v_exp_f32 v71, v71\n\t" \
    "v_exp_f32 v72, v72\n\t" \
    "v_cvt_pk_bf16_f32 v99, v70, v71\n\t" \
    "v_exp_f32 v73, v73\n\t" \
    "v_exp_f32 v74, v74\n\t" \
    "v_cvt_pk_bf16_f32 v100, v72, v73\n\t" \
    "s_cmp_ge_u32 1, %[tmin]\n\t" \
    "s_cselect_b32 %[sm], -1, 0\n\t" \
    "v_and_b32 v96, %[sm], v96\n\t" \
    "v_and_b32 v97, %[sm], v97\n\t" \
    "v_and_b32 v98, %[sm], v98\n\t" \
    "v_and_b32 v99, %[sm], v99\n\t" \
    "s_nop 1\n\t" \
    "v_mfma_f32_32x32x16_bf16 v[0:15], v[136:139], v[96:99], v[0:15]\n\t" \
    "v_exp_f32 v75, v75\n\t" \
    "v_exp_f32 v76, v76\n\t" \
    "v_cvt_pk_bf16_f32 v101, v74, v75\n\t" \
    "v_exp_f32 v77, v77\n\t" \
    "v_exp_f32 v78, v78\n\t" \
    "v_exp_f32 v79, v79\n\t" \
    "v_cvt_pk_bf16_f32 v102, v76, v77\n\t" \
    "v_cvt_pk_bf16_f32 v103, v78, v79\n\t" \
    "v_and_b32 v100, %[sm], v100\n\t" \
    "v_and_b32 v101, %[sm], v101\n\t" \
    "v_and_b32 v102, %[sm], v102\n\t" \
    "v_and_b32 v103, %[sm], v103\n\t" \
    "v_mfma_f32_32x32x16_bf16 v[16:31], v[140:143], v[108:111], v[16:31]\n\t" \
    "v_exp_f32 v80, v80\n\t" \
    "v_exp_f32 v81, v81\n\t" \
    "v_exp_f32 v82, v82\n\t" \
    "v_cvt_pk_bf16_f32 v104, v80, v81\n\t" \
    "v_exp_f32 v83, v83\n\t" \
    "v_exp_f32 v84, v84\n\t" \
    "v_cvt_pk_bf16_f32 v105, v82, v83\n\t" \
    "v_exp_f32 v85, v85\n\t" \
    "v_mfma_f32_32x32x16_bf16 v[64:79], v[120:123], v[112:115], v[32:47]\n\t" \
    "v_exp_f32 v86, v86\n\t" \
    "v_cvt_pk_bf16_f32 v106, v84, v85\n\t" \
    "v_exp_f32 v87, v87\n\t" \
    "v_exp_f32 v88, v88\n\t" \
    "v_cvt_pk_bf16_f32 v107, v86, v87\n\t" \
    "v_exp_f32 v89, v89\n\t" \
    "v_exp_f32 v90, v90\n\t" \
    "v_cvt_pk_bf16_f32 v108, v88, v89\n\t" \
    "s_cmp_ge_u32 2, %[tmin]\n\t" \
    "s_cselect_b32 %[sm], -1, 0\n\t" \
    "v_and_b32 v104, %[sm], v104\n\t" \
    "v_and_b32 v105, %[sm], v105\n\t" \
    "v_and_b32 v106, %[sm], v106\n\t" \
    "v_and_b32 v107, %[sm], v107\n\t" \
    "s_nop 0\n\t" \
    "s_waitcnt lgkmcnt(2)\n\t" \
    "v_mfma_f32_32x32x16_bf16 v[16:31], v[124:127], v[104:107], v[16:31]\n\t" \
    "v_exp_f32 v91, v91\n\t" \
    "v_exp_f32 v92, v92\n\t" \
    "v_cvt_pk_bf16_f32 v109, v90, v91\n\t" \
    "v_exp_f32 v93, v93\n\t" \
    "v_exp_f32 v94, v94\n\t" \
    "v_exp_f32 v95, v95\n\t" \
    "v_cvt_pk_bf16_f32 v110, v92, v93\n\t" \
    "v_cvt_pk_bf16_f32 v111, v94, v95\n\t" \
    "v_and_b32 v108, %[sm], v108\n\t" \
    "v_and_b32 v109, %[sm], v109\n\t" \
    "v_and_b32 v110, %[sm], v110\n\t" \
    "v_and_b32 v111, %[sm], v111\n\t" \
    "v_mfma_f32_32x32x16_bf16 v[0:15], v[140:143], v[100:103], v[0:15]\n\t" \
    "s_mov_b64 exec, %[vmask]\n\t" \
    "ds_read_b128 v[136:139], v161 offset:6144\n\t" \
    "ds_read_b128 v[140:143], v161 offset:6656\n\t" \
    "s_mov_b64 exec, -1\n\t" \
    "ds_read_b128 v[120:123], v160 offset:8192\n\t" \
    "v_exp_f32 v64, v64\n\t" \
    "v_exp_f32 v65, v65\n\t" \
    "v_exp_f32 v66, v66\n\t" \
    "v_cvt_pk_bf16_f32 v96, v64, v65\n\t" \
    "v_exp_f32 v67, v67\n\t" \
    "v_exp_f32 v68, v68\n\t" \
    "v_cvt_pk_bf16_f32 v97, v66, v67\n\t" \
    "v_exp_f32 v69, v69\n\t" \
    "s_waitcnt lgkmcnt(3)\n\t" \
    "v_mfma_f32_32x32x16_bf16 v[80:95], v[132:135], v[116:119], v[48:63]\n\t" \
    "v_exp_f32 v70, v70\n\t" \
    "v_cvt_pk_bf16_f32 v98, v68, v69\n\t" \
    "v_exp_f32 v71, v71\n\t" \
    "v_exp_f32 v72, v72\n\t" \
    "v_cvt_pk_bf16_f32 v99, v70, v71\n\t" \
    "v_exp_f32 v73, v73\n\t" \
    "v_exp_f32 v74, v74\n\t" \
    "v_cvt_pk_bf16_f32 v100, v72, v73\n\t" \
    "s_cmp_ge_u32 2, %[tmin]\n\t" \
    "s_cselect_b32 %[sm], -1, 0\n\t" \
    "v_and_b32 v96, %[sm], v96\n\t" \
    "v_and_b32 v97, %[sm], v97\n\t" \
    "v_and_b32 v98, %[sm], v98\n\t" \
    "v_and_b32 v99, %[sm], v99\n\t" \
    "s_nop 1\n\t" \
    "v_mfma_f32_32x32x16_bf16 v[0:15], v[124:127], v[96:99], v[0:15]\n\t" \
    "v_exp_f32 v75, v75\n\t" \
    "v_exp_f32 v76, v76\n\t" \
    "v_cvt_pk_bf16_f32 v101, v74, v75\n\t" \
    "v_exp_f32 v77, v77\n\t" \
    "v_exp_f32 v78, v78\n\t" \
    "v_exp_f32 v79, v79\n\t" \
    "v_cvt_pk_bf16_f32 v102, v76, v77\n\t" \
    "v_cvt_pk_bf16_f32 v103, v78, v79\n\t" \
    "v_and_b32 v100, %[sm], v100\n\t" \
    "v_and_b32 v101, %[sm], v101\n\t" \
    "v_and_b32 v102, %[sm], v102\n\t" \
    "v_and_b32 v103, %[sm], v103\n\t" \
    "v_mfma_f32_32x32x16_bf16 v[16:31], v[128:131], v[108:111], v[16:31]\n\t" \
    "v_exp_f32 v80, v80\n\t" \
    "v_exp_f32 v81, v81\n\t" \
    "v_exp_f32 v82, v82\n\t" \
    "v_cvt_pk_bf16_f32 v104, v80, v81\n\t" \
    "v_exp_f32 v83, v83\n\t" \
    "v_exp_f32 v84, v84\n\t" \
    "v_cvt_pk_bf16_f32 v105, v82, v83\n\t" \
    "v_exp_f32 v85, v85\n\t" \
    "v_mfma_f32_32x32x16_bf16 v[64:79], v[132:135], v[112:115], v[32:47]\n\t" \
    "v_exp_f32 v86, v86\n\t" \
    "v_cvt_pk_bf16_f32 v106, v84, v85\n\t" \
    "v_exp_f32 v87, v87\n\t" \
    "v_exp_f32 v88, v88\n\t" \
    "v_cvt_pk_bf16_f32 v107, v86, v87\n\t" \
    "v_exp_f32 v89, v89\n\t" \
    "v_exp_f32 v90, v90\n\t" \
    "v_cvt_pk_bf16_f32 v108, v88, v89\n\t" \
    "s_cmp_ge_u32 3, %[tmin]\n\t" \
    "s_cselect_b32 %[sm], -1, 0\n\t" \
    "v_and_b32 v104, %[sm], v104\n\t" \
    "v_and_b32 v105, %[sm], v105\n\t" \
    "v_and_b32 v106, %[sm], v106\n\t" \
    "v_and_b32 v107, %[sm], v107\n\t" \
    "s_nop 0\n\t" \
    "s_waitcnt lgkmcnt(2)\n\t" \
    "v_mfma_f32_32x32x16_bf16 v[16:31], v[136:139], v[104:107], v[16:31]\n\t" \
    "v_exp_f32 v91, v91\n\t" \
    "v_exp_f32 v92, v92\n\t" \
    "v_cvt_pk_bf16_f32 v109, v90, v91\n\t" \
    "v_exp_f32 v93, v93\n\t" \
    "v_exp_f32 v94, v94\n\t" \
    "v_exp_f32 v95, v95\n\t" \
    "v_cvt_pk_bf16_f32 v110, v92, v93\n\t" \
    "v_cvt_pk_bf16_f32 v111, v94, v95\n\t" \
    "v_and_b32 v108, %[sm], v108\n\t" \
    "v_and_b32 v109, %[sm], v109\n\t" \
    "v_and_b32 v110, %[sm], v110\n\t" \
    "v_and_b32 v111, %[sm], v111\n\t" \
    "v_mfma_f32_32x32x16_bf16 v[0:15], v[128:131], v[100:103], v[0:15]\n\t" \
    "s_mov_b64 exec, %[vmask]\n\t" \
    "ds_read_b128 v[124:127], v161 offset:8192\n\t" \
    "ds_read_b128 v[128:131], v161 offset:8704\n\t" \
    "s_mov_b64 exec, -1\n\t" \
    "ds_read_b128 v[132:135], v160 offset:10240\n\t" \
    "v_exp_f32 v64, v64\n\t" \
    "v_exp_f32 v65, v65\n\t" \
    "v_exp_f32 v66, v66\n\t" \
    "v_cvt_pk_bf16_f32 v96, v64, v65\n\t" \
    "v_exp_f32 v67, v67\n\t" \
    "v_exp_f32 v68, v68\n\t" \
    "v_cvt_pk_bf16_f32 v97, v66, v67\n\t" \
    "v_exp_f32 v69, v69\n\t" \
    "s_waitcnt lgkmcnt(3)\n\t" \
    "v_mfma_f32_32x32x16_bf16 v[80:95], v[120:123], v[116:119], v[48:63]\n\t" \
    "v_exp_f32 v70, v70\n\t" \
    "v_cvt_pk_bf16_f32 v98, v68, v69\n\t" \
    "v_exp_f32 v71, v71\n\t" \
    "v_exp_f32 v72, v72\n\t" \
    "v_cvt_pk_bf16_f32 v99, v70, v71\n\t" \
    "v_exp_f32 v73, v73\n\t" \
    "v_exp_f32 v74, v74\n\t" \
    "v_cvt_pk_bf16_f32 v100, v72, v73\n\t" \
    "s_cmp_ge_u32 3, %[tmin]\n\t" \
    "s_cselect_b32 %[sm], -1, 0\n\t" \
    "v_and_b32 v96, %[sm], v96\n\t" \
    "v_and_b32 v97, %[sm], v97\n\t" \
    "v_and_b32 v98, %[sm], v98\n\t" \
    "v_and_b32 v99, %[sm], v99\n\t" \
    "s_nop 1\n\t" \
    "v_mfma_f32_32x32x16_bf16 v[0:15], v[136:139], v[96:99], v[0:15]\n\t" \
    "v_exp_f32 v75, v75\n\t" \
    "v_exp_f32 v76, v76\n\t" \
    "v_cvt_pk_bf16_f32 v101, v74, v75\n\t" \
    "v_exp_f32 v77, v77\n\t" \
    "v_exp_f32 v78, v78\n\t" \
    "v_exp_f32 v79, v79\n\t" \
    "v_cvt_pk_bf16_f32 v102, v76, v77\n\t" \
    "v_cvt_pk_bf16_f32 v103, v78, v79\n\t" \
    "v_and_b32 v100, %[sm], v100\n\t" \
    "v_and_b32 v101, %[sm], v101\n\t" \
    "v_and_b32 v102, %[sm], v102\n\t" \
    "v_and_b32 v103, %[sm], v103\n\t" \
    "v_mfma_f32_32x32x16_bf16 v[16:31], v[140:143], v[108:111], v[16:31]\n\t" \
    "v_exp_f32 v80, v80\n\t" \
    "v_exp_f32 v81, v81\n\t" \
    "v_exp_f32 v82, v82\n\t" \
    "v_cvt_pk_bf16_f32 v104, v80, v81\n\t" \
    "v_exp_f32 v83, v83\n\t" \
    "v_exp_f32 v84, v84\n\t" \
    "v_cvt_pk_bf16_f32 v105, v82, v83\n\t" \
    "v_exp_f32 v85, v85\n\t" \
    "v_mfma_f32_32x32x16_bf16 v[64:79], v[120:123], v[112:115], v[32:47]\n\t" \
    "v_exp_f32 v86, v86\n\t" \
    "v_cvt_pk_bf16_f32 v106, v84, v85\n\t" \
    "v_exp_f32 v87, v87\n\t" \
    "v_exp_f32 v88, v88\n\t" \
    "v_cvt_pk_bf16_f32 v107, v86, v87\n\t" \
    "v_exp_f32 v89, v89\n\t" \
    "v_exp_f32 v90, v90\n\t" \
    "v_cvt_pk_bf16_f32 v108, v88, v89\n\t" \
    "s_cmp_ge_u32 4, %[tmin]\n\t" \
    "s_cselect_b32 %[sm], -1, 0\n\t" \
    "v_and_b32 v104, %[sm], v104\n\t" \
    "v_and_b32 v105, %[sm], v105\n\t" \
    "v_and_b32 v106, %[sm], v106\n\t" \
    "v_and_b32 v107, %[sm], v107\n\t" \
    "s_nop 0\n\t" \
    "s_waitcnt lgkmcnt(2)\n\t" \
    "v_mfma_f32_32x32x16_bf16 v[16:31], v[124:127], v[104:107], v[16:31]\n\t" \
    "v_exp_f32 v91, v91\n\t" \
    "v_exp_f32 v92, v92\n\t" \
    "v_cvt_pk_bf16_f32 v109, v90, v91\n\t" \
    "v_exp_f32 v93, v93\n\t" \
    "v_exp_f32 v94, v94\n\t" \
    "v_exp_f32 v95, v95\n\t" \
    "v_cvt_pk_bf16_f32 v110, v92, v93\n\t" \
    "v_cvt_pk_bf16_f32 v111, v94, v95\n\t" \
    "v_and_b32 v108, %[sm], v108\n\t" \
    "v_and_b32 v109, %[sm], v109\n\t" \
    "v_and_b32 v110, %[sm], v110\n\t" \
    "v_and_b32 v111, %[sm], v111\n\t" \
    "v_mfma_f32_32x32x16_bf16 v[0:15], v[140:143], v[100:103], v[0:15]\n\t" \
    "s_mov_b64 exec, %[vmask]\n\t" \
    "ds_read_b128 v[136:139], v161 offset:10240\n\t" \
    "ds_read_b128 v[140:143], v161 offset:10752\n\t" \
    "s_mov_b64 exec, -1\n\t" \
    "ds_read_b128 v[120:123], v160 offset:12288\n\t" \
    "v_exp_f32 v64, v64\n\t" \
    "v_exp_f32 v65, v65\n\t" \
    "v_exp_f32 v66, v66\n\t" \
    "v_cvt_pk_bf16_f32 v96, v64, v65\n\t" \
    "v_exp_f32 v67, v67\n\t" \
    "v_exp_f32 v68, v68\n\t" \
    "v_cvt_pk_bf16_f32 v97, v66, v67\n\t" \
    "v_exp_f32 v69, v69\n\t" \
    "s_waitcnt lgkmcnt(3)\n\t" \
    "v_mfma_f32_32x32x16_bf16 v[80:95], v[132:135], v[116:119], v[48:63]\n\t" \
    "v_exp_f32 v70, v70\n\t" \
    "v_cvt_pk_bf16_f32 v98, v68, v69\n\t" \
    "v_exp_f32 v71, v71\n\t" \
    "v_exp_f32 v72, v72\n\t" \
    "v_cvt_pk_bf16_f32 v99, v70, v71\n\t" \
    "v_exp_f32 v73, v73\n\t" \
    "v_exp_f32 v74, v74\n\t" \
    "v_cvt_pk_bf16_f32 v100, v72, v73\n\t" \
    "s_cmp_ge_u32 4, %[tmin]\n\t" \
    "s_cselect_b32 %[sm], -1, 0\n\t" \
    "v_and_b32 v96, %[sm], v96\n\t" \
    "v_and_b32 v97, %[sm], v97\n\t" \
    "v_and_b32 v98, %[sm], v98\n\t" \
    "v_and_b32 v99, %[sm], v99\n\t" \
    "s_nop 1\n\t" \
    "v_mfma_f32_32x32x16_bf16 v[0:15], v[124:127], v[96:99], v[0:15]\n\t" \
    "v_exp_f32 v75, v75\n\t" \
    "v_exp_f32 v76, v76\n\t" \
    "v_cvt_pk_bf16_f32 v101, v74, v75\n\t" \
    "v_exp_f32 v77, v77\n\t" \
    "v_exp_f32 v78, v78\n\t" \
    "v_exp_f32 v79, v79\n\t" \
    "v_cvt_pk_bf16_f32 v102, v76, v77\n\t" \
    "v_cvt_pk_bf16_f32 v103, v78, v79\n\t" \
    "v_and_b32 v100, %[sm], v100\n\t" \
    "v_and_b32 v101, %[sm], v101\n\t" \
    "v_and_b32 v102, %[sm], v102\n\t" \
    "v_and_b32 v103, %[sm], v103\n\t" \
    "v_mfma_f32_32x32x16_bf16 v[16:31], v[128:131], v[108:111], v[16:31]\n\t" \
    "v_exp_f32 v80, v80\n\t" \
    "v_exp_f32 v81, v81\n\t" \
    "v_exp_f32 v82, v82\n\t" \
    "v_cvt_pk_bf16_f32 v104, v80, v81\n\t" \
    "v_exp_f32 v83, v83\n\t" \
    "v_exp_f32 v84, v84\n\t" \
    "v_cvt_pk_bf16_f32 v105, v82, v83\n\t" \
    "v_exp_f32 v85, v85\n\t" \
    "v_mfma_f32_32x32x16_bf16 v[64:79], v[132:135], v[112:115], v[32:47]\n\t" \
    "v_exp_f32 v86, v86\n\t" \
    "v_cvt_pk_bf16_f32 v106, v84, v85\n\t" \
    "v_exp_f32 v87, v87\n\t" \
    "v_exp_f32 v88, v88\n\t" \
    "v_cvt_pk_bf16_f32 v107, v86, v87\n\t" \
    "v_exp_f32 v89, v89\n\t" \
    "v_exp_f32 v90, v90\n\t" \
    "v_cvt_pk_bf16_f32 v108, v88, v89\n\t" \
    "s_cmp_ge_u32 5, %[tmin]\n\t" \
    "s_cselect_b32 %[sm], -1, 0\n\t" \
    "v_and_b32 v104, %[sm], v104\n\t" \
    "v_and_b32 v105, %[sm], v105\n\t" \
    "v_and_b32 v106, %[sm], v106\n\t" \
    "v_and_b32 v107, %[sm], v107\n\t" \
    "s_nop 0\n\t" \
    "s_waitcnt lgkmcnt(2)\n\t" \
    "v_mfma_f32_32x32x16_bf16 v[16:31], v[136:139], v[104:107], v[16:31]\n\t" \
    "v_exp_f32 v91, v91\n\t" \
    "v_exp_f32 v92, v92\n\t" \
    "v_cvt_pk_bf16_f32 v109, v90, v91\n\t" \
    "v_exp_f32 v93, v93\n\t" \
    "v_exp_f32 v94, v94\n\t" \
    "v_exp_f32 v95, v95\n\t" \
    "v_cvt_pk_bf16_f32 v110, v92, v93\n\t" \
    "v_cvt_pk_bf16_f32 v111, v94, v95\n\t" \
    "v_and_b32 v108, %[sm], v108\n\t" \
    "v_and_b32 v109, %[sm], v109\n\t" \
    "v_and_b32 v110, %[sm], v110\n\t" \
    "v_and_b32 v111, %[sm], v111\n\t" \
    "v_mfma_f32_32x32x16_bf16 v[0:15], v[128:131], v[100:103], v[0:15]\n\t" \
    "s_mov_b64 exec, %[vmask]\n\t" \
    "ds_read_b128 v[124:127], v161 offset:12288\n\t" \
    "ds_read_b128 v[128:131], v161 offset:12800\n\t" \
    "s_mov_b64 exec, -1\n\t" \
    "ds_read_b128 v[132:135], v160 offset:14336\n\t" \
    "v_exp_f32 v64, v64\n\t" \
    "v_exp_f32 v65, v65\n\t" \
    "v_exp_f32 v66, v66\n\t" \
    "v_cvt_pk_bf16_f32 v96, v64, v65\n\t" \
    "v_exp_f32 v67, v67\n\t" \
    "v_exp_f32 v68, v68\n\t" \
    "v_cvt_pk_bf16_f32 v97, v66, v67\n\t" \
    "v_exp_f32 v69, v69\n\t" \
    "s_waitcnt lgkmcnt(3)\n\t" \
    "v_mfma_f32_32x32x16_bf16 v[80:95], v[120:123], v[116:119], v[48:63]\n\t" \
    "v_exp_f32 v70, v70\n\t" \
    "v_cvt_pk_bf16_f32 v98, v68, v69\n\t" \
    "v_exp_f32 v71, v71\n\t" \
    "v_exp_f32 v72, v72\n\t" \
    "v_cvt_pk_bf16_f32 v99, v70, v71\n\t" \
    "v_exp_f32 v73, v73\n\t" \
    "v_exp_f32 v74, v74\n\t" \
    "v_cvt_pk_bf16_f32 v100, v72, v73\n\t" \
    "s_cmp_ge_u32 5, %[tmin]\n\t" \
    "s_cselect_b32 %[sm], -1, 0\n\t" \
    "v_and_b32 v96, %[sm], v96\n\t" \
    "v_and_b32 v97, %[sm], v97\n\t" \
    "v_and_b32 v98, %[sm], v98\n\t" \
    "v_and_b32 v99, %[sm], v99\n\t" \
    "s_nop 1\n\t" \
    "v_mfma_f32_32x32x16_bf16 v[0:15], v[136:139], v[96:99], v[0:15]\n\t" \
    "v_exp_f32 v75, v75\n\t" \
    "v_exp_f32 v76, v76\n\t" \
    "v_cvt_pk_bf16_f32 v101, v74, v75\n\t" \
    "v_exp_f32 v77, v77\n\t" \
    "v_exp_f32 v78, v78\n\t" \
    "v_exp_f32 v79, v79\n\t" \
    "v_cvt_pk_bf16_f32 v102, v76, v77\n\t" \
    "v_cvt_pk_bf16_f32 v103, v78, v79\n\t" \
    "v_and_b32 v100, %[sm], v100\n\t" \
    "v_and_b32 v101, %[sm], v101\n\t" \
    "v_and_b32 v102, %[sm], v102\n\t" \
    "v_and_b32 v103, %[sm], v103\n\t" \
    "v_mfma_f32_32x32x16_bf16 v[16:31], v[140:143], v[108:111], v[16:31]\n\t" \
    "v_exp_f32 v80, v80\n\t" \
    "v_exp_f32 v81, v81\n\t" \
    "v_exp_f32 v82, v82\n\t" \
    "v_cvt_pk_bf16_f32 v104, v80, v81\n\t" \
    "v_exp_f32 v83, v83\n\t" \
    "v_exp_f32 v84, v84\n\t" \
    "v_cvt_pk_bf16_f32 v105, v82, v83\n\t" \
    "v_exp_f32 v85, v85\n\t" \
    "v_mfma_f32_32x32x16_bf16 v[64:79], v[120:123], v[112:115], v[32:47]\n\t" \
    "v_exp_f32 v86, v86\n\t" \
    "v_cvt_pk_bf16_f32 v106, v84, v85\n\t" \
    "v_exp_f32 v87, v87\n\t" \
    "v_exp_f32 v88, v88\n\t" \
    "v_cvt_pk_bf16_f32 v107, v86, v87\n\t" \
    "v_exp_f32 v89, v89\n\t" \
    "v_exp_f32 v90, v90\n\t" \
    "v_cvt_pk_bf16_f32 v108, v88, v89\n\t" \
    "s_cmp_ge_u32 6, %[tmin]\n\t" \
    "s_cselect_b32 %[sm], -1, 0\n\t" \
    "v_and_b32 v104, %[sm], v104\n\t" \
    "v_and_b32 v105, %[sm], v105\n\t" \
    "v_and_b32 v106, %[sm], v106\n\t" \
    "v_and_b32 v107, %[sm], v107\n\t" \
    "s_nop 0\n\t" \
    "s_waitcnt lgkmcnt(2)\n\t" \
    "v_mfma_f32_32x32x16_bf16 v[16:31], v[124:127], v[104:107], v[16:31]\n\t" \
    "v_exp_f32 v91, v91\n\t" \
    "v_exp_f32 v92, v92\n\t" \
    "v_cvt_pk_bf16_f32 v109, v90, v91\n\t" \
    "v_exp_f32 v93, v93\n\t" \
    "v_exp_f32 v94, v94\n\t" \
    "v_exp_f32 v95, v95\n\t" \
    "v_cvt_pk_bf16_f32 v110, v92, v93\n\t" \
    "v_cvt_pk_bf16_f32 v111, v94, v95\n\t" \
    "v_and_b32 v108, %[sm], v108\n\t" \
    "v_and_b32 v109, %[sm], v109\n\t" \
    "v_and_b32 v110, %[sm], v110\n\t" \
    "v_and_b32 v111, %[sm], v111\n\t" \
    "v_mfma_f32_32x32x16_bf16 v[0:15], v[140:143], v[100:103], v[0:15]\n\t" \
    "s_mov_b64 exec, %[vmask]\n\t" \
    "ds_read_b128 v[136:139], v161 offset:14336\n\t" \
    "ds_read_b128 v[140:143], v161 offset:14848\n\t" \
    "s_mov_b64 exec, -1\n\t" \
    "ds_read_b128 v[120:123], v160 offset:16384\n\t" \
    "v_exp_f32 v64, v64\n\t" \
    "v_exp_f32 v65, v65\n\t" \
    "v_exp_f32 v66, v66\n\t" \
    "v_cvt_pk_bf16_f32 v96, v64, v65\n\t" \
    "v_exp_f32 v67, v67\n\t" \
    "v_exp_f32 v68, v68\n\t" \
    "v_cvt_pk_bf16_f32 v97, v66, v67\n\t" \
    "v_exp_f32 v69, v69\n\t" \
    "s_waitcnt lgkmcnt(3)\n\t" \
    "v_mfma_f32_32x32x16_bf16 v[80:95], v[132:135], v[116:119], v[48:63]\n\t" \
    "v_exp_f32 v70, v70\n\t" \
    "v_cvt_pk_bf16_f32 v98, v68, v69\n\t" \
    "v_exp_f32 v71, v71\n\t" \
    "v_exp_f32 v72, v72\n\t" \
    "v_cvt_pk_bf16_f32 v99, v70, v71\n\t" \
    "v_exp_f32 v73, v73\n\t" \
    "v_exp_f32 v74, v74\n\t" \
    "v_cvt_pk_bf16_f32 v100, v72, v73\n\t" \
    "s_cmp_ge_u32 6, %[tmin]\n\t" \
    "s_cselect_b32 %[sm], -1, 0\n\t" \
    "v_and_b32 v96, %[sm], v96\n\t" \
    "v_and_b32 v97, %[sm], v97\n\t" \
    "v_and_b32 v98, %[sm], v98\n\t" \
    "v_and_b32 v99, %[sm], v99\n\t" \
    "s_nop 1\n\t" \
    "v_mfma_f32_32x32x16_bf16 v[0:15], v[124:127], v[96:99], v[0:15]\n\t" \
    "v_exp_f32 v75, v75\n\t" \
    "v_exp_f32 v76, v76\n\t" \
    "v_cvt_pk_bf16_f32 v101, v74, v75\n\t" \
    "v_exp_f32 v77, v77\n\t" \
    "v_exp_f32 v78, v78\n\t" \
    "v_exp_f32 v79, v79\n\t" \
    "v_cvt_pk_bf16_f32 v102, v76, v77\n\t" \
    "v_cvt_pk_bf16_f32 v103, v78, v79\n\t" \
    "v_and_b32 v100, %[sm], v100\n\t" \
    "v_and_b32 v101, %[sm], v101\n\t" \
    "v_and_b32 v102, %[sm], v102\n\t" \
    "v_and_b32 v103, %[sm], v103\n\t" \
    "v_mfma_f32_32x32x16_bf16 v[16:31], v[128:131], v[108:111], v[16:31]\n\t" \
    "v_exp_f32 v80, v80\n\t" \
    "v_exp_f32 v81, v81\n\t" \
    "v_exp_f32 v82, v82\n\t" \
    "v_cvt_pk_bf16_f32 v104, v80, v81\n\t" \
    "v_exp_f32 v83, v83\n\t" \
    "v_exp_f32 v84, v84\n\t" \
    "v_cvt_pk_bf16_f32 v105, v82, v83\n\t" \
    "v_exp_f32 v85, v85\n\t" \
    "v_mfma_f32_32x32x16_bf16 v[64:79], v[132:135], v[112:115], v[32:47]\n\t" \
    "v_exp_f32 v86, v86\n\t" \
    "v_cvt_pk_bf16_f32 v106, v84, v85\n\t" \
    "v_exp_f32 v87, v87\n\t" \
    "v_exp_f32 v88, v88\n\t" \
    "v_cvt_pk_bf16_f32 v107, v86, v87\n\t" \
    "v_exp_f32 v89, v89\n\t" \
    "v_exp_f32 v90, v90\n\t" \
    "v_cvt_pk_bf16_f32 v108, v88, v89\n\t" \
    "s_cmp_ge_u32 7, %[tmin]\n\t" \
    "s_cselect_b32 %[sm], -1, 0\n\t" \
    "v_and_b32 v104, %[sm], v104\n\t" \
    "v_and_b32 v105, %[sm], v105\n\t" \
    "v_and_b32 v106, %[sm], v106\n\t" \
    "v_and_b32 v107, %[sm], v107\n\t" \
    "s_nop 0\n\t" \
    "s_waitcnt lgkmcnt(2)\n\t" \
    "v_mfma_f32_32x32x16_bf16 v[16:31], v[136:139], v[104:107], v[16:31]\n\t" \
    "v_exp_f32 v91, v91\n\t" \
    "v_exp_f32 v92, v92\n\t" \
    "v_cvt_pk_bf16_f32 v109, v90, v91\n\t" \
    "v_exp_f32 v93, v93\n\t" \
    "v_exp_f32 v94, v94\n\t" \
    "v_exp_f32 v95, v95\n\t" \
    "v_cvt_pk_bf16_f32 v110, v92, v93\n\t" \
    "v_cvt_pk_bf16_f32 v111, v94, v95\n\t" \
    "v_and_b32 v108, %[sm], v108\n\t" \
    "v_and_b32 v109, %[sm], v109\n\t" \
    "v_and_b32 v110, %[sm], v110\n\t" \
    "v_and_b32 v111, %[sm], v111\n\t" \
    "v_mfma_f32_32x32x16_bf16 v[0:15], v[128:131], v[100:103], v[0:15]\n\t" \
    "s_mov_b64 exec, %[vmask]\n\t" \
    "ds_read_b128 v[124:127], v161 offset:16384\n\t" \
    "ds_read_b128 v[128:131], v161 offset:16896\n\t" \
    "s_mov_b64 exec, -1\n\t" \
    "ds_read_b128 v[132:135], v160 offset:18432\n\t" \
    "v_exp_f32 v64, v64\n\t" \
    "v_exp_f32 v65, v65\n\t" \
    "v_exp_f32 v66, v66\n\t" \
    "v_cvt_pk_bf16_f32 v96, v64, v65\n\t" \
    "v_exp_f32 v67, v67\n\t" \
    "v_exp_f32 v68, v68\n\t" \
    "v_cvt_pk_bf16_f32 v97, v66, v67\n\t" \
    "v_exp_f32 v69, v69\n\t" \
    "s_waitcnt lgkmcnt(3)\n\t" \
    "v_mfma_f32_32x32x16_bf16 v[80:95], v[120:123], v[116:119], v[48:63]\n\t" \
    "v_exp_f32 v70, v70\n\t" \
    "v_cvt_pk_bf16_f32 v98, v68, v69\n\t" \
    "v_exp_f32 v71, v71\n\t" \
    "v_exp_f32 v72, v72\n\t" \
    "v_cvt_pk_bf16_f32 v99, v70, v71\n\t" \
    "v_exp_f32 v73, v73\n\t" \
    "v_exp_f32 v74, v74\n\t" \
    "v_cvt_pk_bf16_f32 v100, v72, v73\n\t" \
    "s_cmp_ge_u32 7, %[tmin]\n\t" \
    "s_cselect_b32 %[sm], -1, 0\n\t" \
    "v_and_b32 v96, %[sm], v96\n\t" \
    "v_and_b32 v97, %[sm], v97\n\t" \
    "v_and_b32 v98, %[sm], v98\n\t" \
    "v_and_b32 v99, %[sm], v99\n\t" \
    "s_nop 1\n\t" \
    "v_mfma_f32_32x32x16_bf16 v[0:15], v[136:139], v[96:99], v[0:15]\n\t" \
    "v_exp_f32 v75, v75\n\t" \
    "v_exp_f32 v76, v76\n\t" \
    "v_cvt_pk_bf16_f32 v101, v74, v75\n\t" \
    "v_exp_f32 v77, v77\n\t" \
    "v_exp_f32 v78, v78\n\t" \
    "v_exp_f32 v79, v79\n\t" \
    "v_cvt_pk_bf16_f32 v102, v76, v77\n\t" \
    "v_cvt_pk_bf16_f32 v103, v78, v79\n\t" \
    "v_and_b32 v100, %[sm], v100\n\t" \
    "v_and_b32 v101, %[sm], v101\n\t" \
    "v_and_b32 v102, %[sm], v102\n\t" \
    "v_and_b32 v103, %[sm], v103\n\t" \
    "v_mfma_f32_32x32x16_bf16 v[16:31], v[140:143], v[108:111], v[16:31]\n\t" \
    "v_exp_f32 v80, v80\n\t" \
    "v_exp_f32 v81, v81\n\t" \
    "v_exp_f32 v82, v82\n\t" \
    "v_cvt_pk_bf16_f32 v104, v80, v81\n\t" \
    "v_exp_f32 v83, v83\n\t" \
    "v_exp_f32 v84, v84\n\t" \
    "v_cvt_pk_bf16_f32 v105, v82, v83\n\t" \
    "v_exp_f32 v85, v85\n\t" \
    "v_mfma_f32_32x32x16_bf16 v[64:79], v[120:123], v[112:115], v[32:47]\n\t" \
    "v_exp_f32 v86, v86\n\t" \
    "v_cvt_pk_bf16_f32 v106, v84, v85\n\t" \
    "v_exp_f32 v87, v87\n\t" \
    "v_exp_f32 v88, v88\n\t" \
    "v_cvt_pk_bf16_f32 v107, v86, v87\n\t" \
    "v_exp_f32 v89, v89\n\t" \
    "v_exp_f32 v90, v90\n\t" \
    "v_cvt_pk_bf16_f32 v108, v88, v89\n\t" \
    "s_waitcnt lgkmcnt(2)\n\t" \
    "v_mfma_f32_32x32x16_bf16 v[16:31], v[124:127], v[104:107], v[16:31]\n\t" \
    "v_exp_f32 v91, v91\n\t" \
    "v_exp_f32 v92, v92\n\t" \
    "v_cvt_pk_bf16_f32 v109, v90, v91\n\t" \
    "v_exp_f32 v93, v93\n\t" \
    "v_exp_f32 v94, v94\n\t" \
    "v_exp_f32 v95, v95\n\t" \
    "v_cvt_pk_bf16_f32 v110, v92, v93\n\t" \
    "v_cvt_pk_bf16_f32 v111, v94, v95\n\t" \
    "v_mfma_f32_32x32x16_bf16 v[0:15], v[140:143], v[100:103], v[0:15]\n\t" \
    "s_mov_b64 exec, %[vmask]\n\t" \
    "ds_read_b128 v[136:139], v161 offset:18432\n\t" \
    "ds_read_b128 v[140:143], v161 offset:18944\n\t" \
    "s_mov_b64 exec, -1\n\t" \
    "v_exp_f32 v64, v64\n\t" \
    "v_exp_f32 v65, v65\n\t" \
    "v_exp_f32 v66, v66\n\t" \
    "v_cvt_pk_bf16_f32 v96, v64, v65\n\t" \
    "v_exp_f32 v67, v67\n\t" \
    "v_exp_f32 v68, v68\n\t" \
    "v_cvt_pk_bf16_f32 v97, v66, v67\n\t" \
    "v_exp_f32 v69, v69\n\t" \
    "s_waitcnt lgkmcnt(2)\n\t" \
    "v_mfma_f32_32x32x16_bf16 v[80:95], v[132:135], v[116:119], v[48:63]\n\t" \
    "v_exp_f32 v70, v70\n\t" \
    "v_cvt_pk_bf16_f32 v98, v68, v69\n\t" \
    "v_exp_f32 v71, v71\n\t" \
    "v_exp_f32 v72, v72\n\t" \
    "v_cvt_pk_bf16_f32 v99, v70, v71\n\t" \
    "v_exp_f32 v73, v73\n\t" \
    "v_exp_f32 v74, v74\n\t" \
    "v_cvt_pk_bf16_f32 v100, v72, v73\n\t" \
    "v_and_b32 v96, v96, v152\n\t" \
    "v_and_b32 v97, v97, v153\n\t" \
    "v_and_b32 v98, v98, v154\n\t" \
    "v_and_b32 v99, v99, v155\n\t" \
    "s_nop 1\n\t" \
    "v_mfma_f32_32x32x16_bf16 v[0:15], v[124:127], v[96:99], v[0:15]\n\t" \
    "v_exp_f32 v75, v75\n\t" \
    "v_exp_f32 v76, v76\n\t" \
    "v_cvt_pk_bf16_f32 v101, v74, v75\n\t" \
    "v_exp_f32 v77, v77\n\t" \
    "v_exp_f32 v78, v78\n\t" \
    "v_exp_f32 v79, v79\n\t" \
    "v_cvt_pk_bf16_f32 v102, v76, v77\n\t" \
    "v_cvt_pk_bf16_f32 v103, v78, v79\n\t" \
    "v_and_b32 v100, v100, v156\n\t" \
    "v_and_b32 v101, v101, v157\n\t" \
    "v_and_b32 v102, v102, v158\n\t" \
    "v_and_b32 v103, v103, v159\n\t" \
    "v_mfma_f32_32x32x16_bf16 v[16:31], v[128:131], v[108:111], v[16:31]\n\t" \
    "v_exp_f32 v80, v80\n\t" \
    "v_exp_f32 v81, v81\n\t" \
    "v_exp_f32 v82, v82\n\t" \
    "v_cvt_pk_bf16_f32 v104, v80, v81\n\t" \
    "v_exp_f32 v83, v83\n\t" \
    "v_exp_f32 v84, v84\n\t" \
    "v_cvt_pk_bf16_f32 v105, v82, v83\n\t" \
    "v_exp_f32 v85, v85\n\t" \
    "v_exp_f32 v86, v86\n\t" \
    "v_cvt_pk_bf16_f32 v106, v84, v85\n\t" \
    "v_exp_f32 v87, v87\n\t" \
    "v_exp_f32 v88, v88\n\t" \
    "v_cvt_pk_bf16_f32 v107, v86, v87\n\t" \
    "v_exp_f32 v89, v89\n\t" \
    "v_exp_f32 v90, v90\n\t" \
    "v_cvt_pk_bf16_f32 v108, v88, v89\n\t" \
    "v_and_b32 v104, v104, v152\n\t" \
    "v_and_b32 v105, v105, v153\n\t" \
    "v_and_b32 v106, v106, v154\n\t" \
    "v_and_b32 v107, v107, v155\n\t" \
    "s_waitcnt lgkmcnt(1)\n\t" \
    "s_nop 0\n\t" \
    "v_mfma_f32_32x32x16_bf16 v[16:31], v[136:139], v[104:107], v[16:31]\n\t" \
    "v_exp_f32 v91, v91\n\t" \
    "v_exp_f32 v92, v92\n\t" \
    "v_cvt_pk_bf16_f32 v109, v90, v91\n\t" \
    "v_exp_f32 v93, v93\n\t" \
    "v_exp_f32 v94, v94\n\t" \
    "v_exp_f32 v95, v95\n\t" \
    "v_cvt_pk_bf16_f32 v110, v92, v93\n\t" \
    "v_cvt_pk_bf16_f32 v111, v94, v95\n\t" \
    "v_and_b32 v108, v108, v156\n\t" \
    "v_and_b32 v109, v109, v157\n\t" \
    "v_and_b32 v110, v110, v158\n\t" \
    "v_and_b32 v111, v111, v159\n\t" \
    "v_mfma_f32_32x32x16_bf16 v[0:15], v[128:131], v[100:103], v[0:15]\n\t" \
    "s_waitcnt lgkmcnt(0)\n\t" \
    "s_nop 1\n\t" \
    "v_mfma_f32_32x32x16_bf16 v[16:31], v[140:143], v[108:111], v[16:31]\n\t" \
    "s_nop 15\n\t"

#define ATTN_A_CLOB "v64","v65","v66","v67","v68","v69","v70","v71","v72","v73","v74","v75","v76","v77","v78","v79","v80","v81","v82","v83","v84","v85","v86","v87","v88","v89","v90","v91","v92","v93","v94","v95","v96","v97","v98","v99","v100","v101","v102","v103","v104","v105","v106","v107","v108","v109","v110","v111","v120","v121","v122","v123","v124","v125","v126","v127","v128","v129","v130","v131","v132","v133","v134","v135"
#define ATTN_B_CLOB "v64","v65","v66","v67","v68","v69","v70","v71","v72","v73","v74","v75","v76","v77","v78","v79","v80","v81","v82","v83","v84","v85","v86","v87","v88","v89","v90","v91","v92","v93","v94","v95","v96","v97","v98","v99","v100","v101","v102","v103","v104","v105","v106","v107","v108","v109","v110","v111","v120","v121","v122","v123","v124","v125","v126","v127","v128","v129","v130","v131","v132","v133","v134","v135","v136","v137","v138","v139","v140","v141","v142","v143"

typedef short bf16x8 __attribute__((ext_vector_type(8)));
typedef short s16x4 __attribute__((ext_vector_type(4)));
typedef float f32x16 __attribute__((ext_vector_type(16)));
typedef float f32x2 __attribute__((ext_vector_type(2)));
typedef __bf16 b16x2 __attribute__((ext_vector_type(2)));
typedef unsigned u32x4 __attribute__((ext_vector_type(4)));
typedef unsigned u32x2 __attribute__((ext_vector_type(2)));
typedef unsigned long long u64;
typedef const __attribute__((address_space(3))) char* lds_cptr;
#define LDSP(T) __attribute__((address_space(3))) T*
#define GASP(T) __attribute__((address_space(1))) T*
#define DI __device__ __forceinline__
#define MFMA(a, b, c) __builtin_amdgcn_mfma_f32_32x32x16_bf16(a, b, c, 0, 0, 0)
#define WAIT_VM(n) asm volatile("s_waitcnt vmcnt(" #n ")" ::: "memory")
#define BARRIER() asm volatile("s_waitcnt lgkmcnt(0)\n\ts_barrier" ::: "memory")

constexpr int SEQ = 2048;
constexpr unsigned WS_FQ = 0x1000, WS_FO = 0x8000, WS_Q = 0x100000, WS_KV = 0x500000, WS_O = 0xD00000, WS_BYTES = 0x1100000;
constexpr unsigned Q_BR = 0x200000, KV_BR = 0x400000, Q_HEAD = 0x10000, KV_HEAD = 0x20000;
constexpr float QSCALE = 0.25f * 1.4426950408889634f;
constexpr int STAGE = 32768, LDS_SPARE = 131072, LDS_BYTES = 163840;
constexpr unsigned PROG_MAGIC = 0x7FC1F00Du;
constexpr int LDS_WO = 2048, LDS_XCH = 69632;
constexpr unsigned SPIN_CAP = 1u << 20;

DI unsigned cvtpk(float lo, float hi) { f32x2 v = {lo, hi}; b16x2 b = __builtin_convertvector(v, b16x2); return __builtin_bit_cast(unsigned, b); }
template <int SC1> DI void glds16(const void* sbase, unsigned voff, unsigned lds_dst) {
    unsigned keep;
    if (SC1) asm volatile("s_mov_b32 %0, m0\n\ts_mov_b32 m0, %3\n\ts_nop 0\n\tglobal_load_lds_dwordx4 %1, %2 sc1\n\ts_mov_b32 m0, %0" : "=&s"(keep) : "v"(voff), "s"(sbase), "s"(lds_dst) : "memory");
    else asm volatile("s_mov_b32 %0, m0\n\ts_mov_b32 m0, %3\n\ts_nop 0\n\tglobal_load_lds_dwordx4 %1, %2\n\ts_mov_b32 m0, %0" : "=&s"(keep) : "v"(voff), "s"(sbase), "s"(lds_dst) : "memory");
}
DI void dma_stage(const char* sx, const char* sw, unsigned dx, unsigned dw, unsigned vx0, unsigned vx1, unsigned vw0, unsigned vw1, unsigned vw2) {
    unsigned keep; const char* sw2 = sw + 8 * 3072;
    asm volatile("s_mov_b32 %0, m0\n\ts_mov_b32 m0, %1\n\ts_nop 0\n\t"
                 "global_load_lds_dwordx4 %6, %3\n\tglobal_load_lds_dwordx4 %7, %3 offset:1024\n\t"
                 "s_mov_b32 m0, %2\n\ts_nop 0\n\t"
                 "global_load_lds_dwordx4 %8, %4\n\tglobal_load_lds_dwordx4 %9, %4 offset:1024\n\tglobal_load_lds_dwordx4 %10, %4 offset:2048\n\t"
                 "s_add_u32 m0, m0, 0xc00\n\ts_nop 0\n\t"
                 "global_load_lds_dwordx4 %8, %5\n\tglobal_load_lds_dwordx4 %9, %5 offset:1024\n\tglobal_load_lds_dwordx4 %10, %5 offset:2048\n\t"
                 "s_mov_b32 m0, %0"
                 : "=&s"(keep) : "s"(dx), "s"(dw), "s"(sx), "s"(sw), "s"(sw2), "v"(vx0), "v"(vx1), "v"(vw0), "v"(vw1), "v"(vw2) : "memory", "scc");
}
DI void dma4_sc1(const char* s, unsigned d, unsigned v) {
    unsigned keep;
    asm volatile("s_mov_b32 %0, m0\n\ts_mov_b32 m0, %1\n\ts_nop 0\n\t"
                 "global_load_lds_dwordx4 %3, %2 sc1\n\tglobal_load_lds_dwordx4 %3, %2 offset:1024 sc1\n\tglobal_load_lds_dwordx4 %3, %2 offset:2048 sc1\n\tglobal_load_lds_dwordx4 %3, %2 offset:3072 sc1\n\t"
                 "s_mov_b32 m0, %0" : "=&s"(keep) : "s"(d), "s"(s), "v"(v) : "memory");
}
DI void touch(const void* sbase, unsigned voff, unsigned lds_dst) {
    unsigned keep;
    asm volatile("s_mov_b32 %0, m0\n\ts_mov_b32 m0, %3\n\ts_nop 0\n\tglobal_load_lds_dword %1, %2\n\ts_mov_b32 m0, %0" : "=&s"(keep) : "v"(voff), "s"(sbase), "s"(lds_dst) : "memory");
}
DI bf16x8 lds_b128(lds_cptr p) { return *(const LDSP(bf16x8))p; }
DI s16x4 lds_tr(lds_cptr p) { return __builtin_amdgcn_ds_read_tr16_b64_v4i16((LDSP(s16x4))p); }
DI bf16x8 cat8(s16x4 a, s16x4 b) { return (bf16x8){a[0], a[1], a[2], a[3], b[0], b[1], b[2], b[3]}; }
DI u32x4 pack8(const f32x16& a, int o, float sc) {
    return (u32x4){cvtpk(a[o] * sc, a[o + 1] * sc), cvtpk(a[o + 2] * sc, a[o + 3] * sc), cvtpk(a[o + 4] * sc, a[o + 5] * sc), cvtpk(a[o + 6] * sc, a[o + 7] * sc)};
}
typedef __amdgpu_buffer_rsrc_t rsrc_t;
DI bf16x8 ld_sc1(rsrc_t r, unsigned off) { return __builtin_bit_cast(bf16x8, __builtin_amdgcn_raw_buffer_load_b128(r, off, 0, 16)); }
DI void st_sc1(rsrc_t r, unsigned off, u32x4 v) { __builtin_amdgcn_raw_buffer_store_b128(v, r, off, 0, 16); }
DI u64 flag_ld(const char* ws, unsigned off) { return __hip_atomic_load((GASP(u64))(ws + off), __ATOMIC_RELAXED, __HIP_MEMORY_SCOPE_AGENT); }
DI void flag_st(char* ws, unsigned off, u64 v) { __hip_atomic_store((GASP(u64))(ws + off), v, __ATOMIC_RELAXED, __HIP_MEMORY_SCOPE_AGENT); }
DI void wait_flags(const char* ws, unsigned off, bool active, u64 tag) {
#pragma unroll 1
    for (unsigned it = 0; it < SPIN_CAP; ++it) {
        const u64 v = active ? flag_ld(ws, off) : tag;
        if (__all(v == tag)) break;
        __builtin_amdgcn_s_sleep(1);
    }
    asm volatile("" ::: "memory");
}
DI float rowmax16(const f32x16& s) {
    float a = fmaxf(fmaxf(s[0], s[1]), s[2]), b = fmaxf(fmaxf(s[3], s[4]), s[5]);
    a = fmaxf(fmaxf(a, s[6]), s[7]); b = fmaxf(fmaxf(b, s[8]), s[9]); a = fmaxf(fmaxf(a, s[10]), s[11]); b = fmaxf(fmaxf(b, s[12]), s[13]); a = fmaxf(fmaxf(a, s[14]), s[15]);
    const float m = fmaxf(a, b); auto rr = __builtin_amdgcn_permlane32_swap(__float_as_uint(m), __float_as_uint(m), false, false);
    return fmaxf(__uint_as_float(rr[0]), __uint_as_float(rr[1]));
}
DI f32x16 splat16(float v) { f32x16 r;
#pragma unroll
    for (int i = 0; i < 16; ++i) r[i] = v; return r; }
DI void exp_pack(f32x16& s, bf16x8& p0, bf16x8& p1) {
#pragma unroll
    for (int i = 0; i < 16; ++i) s[i] = __builtin_amdgcn_exp2f(s[i]);
    p0 = __builtin_bit_cast(bf16x8, pack8(s, 0, 1.f)); p1 = __builtin_bit_cast(bf16x8, pack8(s, 8, 1.f));
}
DI bf16x8 vfix(bf16x8 v, bool lo) { const bf16x8 ones = {0x3F80, 0x3F80, 0x3F80, 0x3F80, 0x3F80, 0x3F80, 0x3F80, 0x3F80}; return lo ? v : ones; }

DI f32x16 exact_block(rsrc_t rs, unsigned kv0, bf16x8 qf, int t_lo, int t_hi, int tF, int tL, int lane) {
    const int l31 = lane & 31, hi = lane >> 5; const bool vlo = l31 < 16;
    const unsigned ko = kv0 + lane * 16, vo = kv0 + 1024 + hi * 256 + (l31 & 15) * 16;
    float m = -INFINITY;
#pragma unroll 1
    for (int t = t_lo; t <= t_hi; ++t) {
        f32x16 c;
#pragma unroll
        for (int r = 0; r < 16; ++r) { const int jj = (r & 3) + 8 * (r >> 2) + 4 * hi; c[r] = ((t == tF && jj <= l31) || (t == tL && jj > l31)) ? -INFINITY : 0.f; }
        m = fmaxf(m, rowmax16(MFMA(ld_sc1(rs, ko + t * 2048), qf, c)));
    }
    f32x16 o = {};
#pragma unroll 1
    for (int t = t_lo; t <= t_hi; ++t) {
        f32x16 c;
#pragma unroll
        for (int r = 0; r < 16; ++r) { const int jj = (r & 3) + 8 * (r >> 2) + 4 * hi; c[r] = ((t == tF && jj <= l31) || (t == tL && jj > l31)) ? -INFINITY : -m; }
        f32x16 s = MFMA(ld_sc1(rs, ko + t * 2048), qf, c); bf16x8 p0, p1; exp_pack(s, p0, p1);
        o = MFMA(vfix(ld_sc1(rs, vo + t * 2048), vlo), p0, o); o = MFMA(vfix(ld_sc1(rs, vo + t * 2048 + 512), vlo), p1, o);
    }
    return o;
}
DI bool block_ok(const f32x16& o) {
    bool ok = o[8] < 0x1p100f && o[8] > 0x1p-80f;
#pragma unroll
    for (int r = 0; r < 8; ++r) ok &= fabsf(o[r]) < INFINITY;
    return __all(ok);
}

__global__ __launch_bounds__(512, 2) void fused_kernel(const uint16_t* xA, const uint16_t* wA, const uint16_t* woA, const uint16_t* xB, const uint16_t* wB, const uint16_t* woB, char* ws, uint16_t* out) {
    asm volatile("fused_code_begin:");
    extern __shared__ __attribute__((aligned(16))) char smem[];
    const int bid = blockIdx.x, xc = bid & 7, j = bid >> 3;
    const int lane = threadIdx.x & 63, wv = __builtin_amdgcn_readfirstlane(threadIdx.x >> 6), hi = lane >> 5, l31 = lane & 31;
    const unsigned lds0 = (unsigned)(uintptr_t)(LDSP(char))smem;
    u64 tag;
#define READ_TAG() do { asm volatile("s_load_dwordx2 %0, %1, 0x0\n\ts_waitcnt lgkmcnt(0)" : "=s"(tag) : "s"(ws) : "memory"); tag += 1; } while (0)
    const rsrc_t rs = __builtin_amdgcn_make_buffer_rsrc(ws, 0, WS_BYTES, 0x00020000);
    const int rb = (xc >> 1) * 8 + (j & 7), hg = (xc & 1) * 4 + (j >> 3), c0 = ((j & 7) + 2 * (j >> 3)) & 7;
    const int h = xc * 4 + (j & 3), qb = j >> 2;
    const int obr = xc >> 2, orb = (xc & 3) * 4 + (j & 3), ocb = j >> 2;
    f32x16 oa, ob; bf16x8 qa, qbf; unsigned kv0, o_off, f_off; int tlo_a, tlo_b, thi, tF, tL, bstep;
    if (__builtin_expect(wv >= 4, 1)) {
        const int lw = wv - 4;
        unsigned voffx[2], voffw[3];
#pragma unroll
        for (int m = 0; m < 2; ++m) voffx[m] = (8 * (2 * lw + m) + (lane >> 3)) * 1024 + (((lane & 7) ^ ((4 * m + (lane >> 4)) & 7)) << 4) - m * 1024;
#pragma unroll
        for (int t = 0; t < 3; ++t) { const int off = t * 1024 + lane * 16, r8 = off / 384, c = (off % 384) >> 4; voffw[t] = r8 * 3072 + (c >> 3) * 1024 + hg * 128 + (((c & 7) ^ (((r8 >> 1) & 1) << 2)) << 4) - t * 1024; }
        const char* sx; const char* sw;
        auto prep = [&](int g) {
            const int p = g >> 3, kc = (c0 + g) & 7;
            sx = (const char*)(p ? xB : xA) + (size_t)rb * 64 * 1024 + kc * 128;
            sw = (const char*)(p ? wB : wA) + (size_t)(kc * 64 + 16 * lw) * 3072;
            asm volatile("" : "+s"(sx), "+s"(sw));
        };
        auto stage = [&](int g) { const unsigned d = lds0 + (g % 5) * STAGE; dma_stage(sx, sw, d + lw * 2048, d + 8192 + lw * 6144, voffx[0], voffx[1], voffw[0], voffw[1], voffw[2]); };
        auto kvstage = [&](int s) {
            const char* src = ws + WS_KV + (size_t)h * KV_HEAD + s * STAGE + lw * 8192;
            dma4_sc1(src, lds0 + s * STAGE + lw * 8192, lane * 16); dma4_sc1(src + 4096, lds0 + s * STAGE + lw * 8192 + 4096, lane * 16);
        };
        prep(0); stage(0); prep(1);
        READ_TAG();
        stage(1); prep(2);
        WAIT_VM(8); BARRIER();
        stage(2); prep(3); stage(3); prep(4); stage(4); prep(5);
        WAIT_VM(24); BARRIER();
#pragma unroll
        for (int g = 2; g < 13; ++g) {
            stage(g + 3); if (g + 4 <= 15) prep(g + 4);
            WAIT_VM(24);
            BARRIER();
        }
        WAIT_VM(16); BARRIER();
        WAIT_VM(8); BARRIER();
        const unsigned fqo = WS_FQ + (((h >> 1) * 64) + lane) * 8;
        u64 pollv;
        asm volatile("global_load_dwordx2 %0, %1, %2 sc1\n\ts_waitcnt vmcnt(0)" : "=v"(pollv) : "v"(fqo), "s"(ws) : "memory");
        BARRIER();
        bool seen = __all(pollv == tag);
        if (!seen) seen = __all(flag_ld(ws, fqo) == tag);
        if (lane == 0) ((LDSP(unsigned))(smem + LDS_SPARE))[lw] = seen ? 1u : 0u;
        BARRIER();
        if (!seen) wait_flags(ws, fqo, true, tag);
        kvstage(0); kvstage(1);
        WAIT_VM(8);
        BARRIER();
        kvstage(2); kvstage(3);
        if (lw < 2 && lane < 16) touch((const char*)(lw ? woB : woA) + bid * 2048, lane * 128, lds0 + LDS_SPARE + lw * 256);
        u32x4 mf0, mf1, ml0, ml1;
#pragma unroll
        for (int k = 0; k < 4; ++k) {
            auto w = [&](int r) { const int j0 = (r & 3) + 8 * (r >> 2) + 4 * hi; return (j0 <= l31 ? 0xFFFFu : 0u) | (j0 + 1 <= l31 ? 0xFFFF0000u : 0u); };
            ml0[k] = w(2 * k); ml1[k] = w(8 + 2 * k); mf0[k] = ~ml0[k]; mf1[k] = ~ml1[k];
        }
        asm volatile("" : "+v"(mf0), "+v"(mf1), "+v"(ml0), "+v"(ml1));
        WAIT_VM(0);
        BARRIER();
        if (bid == 0 && wv == 7) {
#pragma unroll 1
            for (unsigned it = 0; it < SPIN_CAP; ++it) {
                bool ok = true;
#pragma unroll
                for (int i = 0; i < 16; ++i) ok &= flag_ld(ws, WS_FQ + (i * 64 + lane) * 8) == tag;
                if (__all(ok)) break;
                __builtin_amdgcn_s_sleep(1);
            }
            if (lane == 0) flag_st(ws, 0, tag);
        }
        {
            const int qw = qb * 256 + 64 * lw, T0 = qw / 32 - 8, tmin = T0 < 0 ? -T0 : 0;
            { const int t = T0 + lane; wait_flags(ws, WS_FQ + ((16 + (h >> 1)) * 64 + t) * 8, lane >= tmin && lane < 10, tag); }
            {
                const char* src = ws + WS_KV + KV_BR + (size_t)h * KV_HEAD + (ptrdiff_t)T0 * 2048; const unsigned dst = lds0 + LDS_SPARE + lw * 4096;
                if (tmin == 0) {
#pragma unroll
                    for (int i = 0; i < 5; ++i) dma4_sc1(src + i * 4096, dst + i * 4096, lane * 16);
                } else {
#pragma unroll 1
                    for (int i = tmin / 2; i < 5; ++i) dma4_sc1(src + i * 4096, dst + i * 4096, lane * 16);
#pragma unroll 1
                    for (int i = 0; i < 2 * tmin; ++i) *(LDSP(u32x4))(smem + LDS_SPARE + lw * 4096 + i * 1024 + lane * 16) = (u32x4){0u, 0u, 0u, 0u};
                }
            }
            const unsigned qofs = WS_Q + Q_BR + h * Q_HEAD + (qw / 32) * 1024 + lane * 16;
            qa = ld_sc1(rs, qofs); qbf = ld_sc1(rs, qofs + 1024);
            WAIT_VM(0);
            __builtin_amdgcn_s_setprio(3);
            lds_cptr kp = (lds_cptr)smem + LDS_SPARE + lw * 4096 + lane * 16;
            const bf16x8 k8 = lds_b128(kp + 8 * 2048);
            float ma;
            if (tmin <= 7) ma = rowmax16(MFMA(lds_b128(kp + 7 * 2048), qa, splat16(0.f)));
            else { f32x16 mL;
#pragma unroll
                for (int r = 0; r < 16; ++r) { const int jj = (r & 3) + 8 * (r >> 2) + 4 * hi; mL[r] = jj <= l31 ? 0.f : -INFINITY; }
                ma = rowmax16(MFMA(k8, qa, mL)); }
            f32x16 ca = splat16(-ma), cb = splat16(-rowmax16(MFMA(k8, qbf, splat16(0.f))));
            unsigned kaddr = lds0 + LDS_SPARE + lw * 4096 + lane * 16, vaddr = lds0 + LDS_SPARE + lw * 4096 + 1024 + hi * 256 + (l31 & 15) * 16;
            unsigned fill = (l31 == 16 || l31 == 20) ? 0x3f803f80u : 0u; const u64 vmask = 0x0000FFFF0000FFFFull;
            oa = f32x16{}; ob = f32x16{};
            __builtin_amdgcn_s_setprio(0);
            if (tmin == 0) {
                asm volatile(ATTN_B_ASM : "+{v[0:15]}"(oa), "+{v[16:31]}"(ob)
                    : "{v[32:47]}"(ca), "{v[48:63]}"(cb), "{v[112:115]}"(qa), "{v[116:119]}"(qbf), "{v[144:147]}"(mf0), "{v[148:151]}"(mf1), "{v[152:155]}"(ml0), "{v[156:159]}"(ml1),
                      "{v160}"(kaddr), "{v161}"(vaddr), "{v162}"(fill), [vmask] "s"(vmask)
                    : ATTN_B_CLOB, "memory");
            } else {
                unsigned sm;
                asm volatile(ATTN_BH_ASM : "+{v[0:15]}"(oa), "+{v[16:31]}"(ob), [sm] "=&s"(sm)
                    : "{v[32:47]}"(ca), "{v[48:63]}"(cb), "{v[112:115]}"(qa), "{v[116:119]}"(qbf), "{v[152:155]}"(ml0), "{v[156:159]}"(ml1),
                      "{v160}"(kaddr), "{v161}"(vaddr), "{v162}"(fill), [vmask] "s"(vmask), [tmin] "s"(tmin)
                    : ATTN_B_CLOB, "scc", "memory");
            }
            kv0 = WS_KV + KV_BR + h * KV_HEAD + T0 * 2048;
            tlo_a = tmin; tlo_b = tmin > 0 ? tmin - 1 : 0; thi = 8; tF = 0; tL = 8; bstep = 2048;
            o_off = WS_O + Q_BR + h * Q_HEAD + (qw / 32) * 1024 + lane * 16; f_off = WS_FO + ((32 + qw / 64) * 32 + h) * 8;
        }
    } else {
        const int tt = wv & 1, hp = wv >> 1, q = (lane >> 2) & 3;
        {
            u64 pc; unsigned len, before;
            asm volatile("s_getpc_b64 %0\nfused_code_mark:\n\ts_mov_b32 %1, fused_code_end - fused_code_mark\n\ts_mov_b32 %2, fused_code_mark - fused_code_begin" : "=s"(pc), "=s"(len), "=s"(before));
            const unsigned off = (j * 16 + (lane & 15) + (wv << 9)) * 128;
            if (lane < 16 && off < before + len) { unsigned dummy; const char* p = (const char*)((pc - before) & ~127ull) + off;
                asm volatile("global_load_dword %0, %1, off\n\ts_waitcnt vmcnt(0)" : "=v"(dummy) : "v"(p) : "memory"); }
        }
        READ_TAG();
        const int xrow = 32 * tt + l31; const unsigned xaddr0 = xrow * 128 + ((hi ^ ((l31 >> 1) & 7)) << 4);
        const unsigned wbase = 8192 + (8 * hi + q) * 384 + ((hp ^ (q >> 1)) * 64) + ((lane >> 4) & 1) * 32 + (lane & 3) * 8;
        f32x16 acc0, acc1, acc2, fx, fw0, fw1, fw2, gx, gw0, gw1, gw2;
        unsigned xb0 = lds0 + xaddr0, xb1 = lds0 + (xaddr0 ^ 32), xb2 = lds0 + (xaddr0 ^ 64), xb3 = lds0 + (xaddr0 ^ 96), wb_ = lds0 + wbase;
        unsigned soff = 0;
        asm volatile(QKV_FIRST_ASM : "={v[48:63]}"(fx), "={v[64:79]}"(fw0), "={v[80:95]}"(fw1), "={v[96:111]}"(fw2), "={v[112:127]}"(gx), "={v[128:143]}"(gw0), "={v[144:159]}"(gw1), "={v[160:175]}"(gw2)
                     : [soff] "s"(soff), "{v176}"(xb0), "{v177}"(xb1), "{v178}"(xb2), "{v179}"(xb3), "{v180}"(wb_) : "v181", "v182", "v183", "v184", "v185", "memory");
#define QKV_STEP(TXT) asm volatile(TXT : "+{v[0:15]}"(acc0), "+{v[16:31]}"(acc1), "+{v[32:47]}"(acc2), "+{v[48:63]}"(fx), "+{v[64:79]}"(fw0), "+{v[80:95]}"(fw1), "+{v[96:111]}"(fw2), \
                         "+{v[112:127]}"(gx), "+{v[128:143]}"(gw0), "+{v[144:159]}"(gw1), "+{v[160:175]}"(gw2), [soff] "+s"(soff) \
                         : "{v176}"(xb0), "{v177}"(xb1), "{v178}"(xb2), "{v179}"(xb3), "{v180}"(wb_) : "v181", "v182", "v183", "v184", "v185", "scc", "memory")
#define QKV_FENCE() asm volatile("s_nop 15" : "+{v[0:15]}"(acc0), "+{v[16:31]}"(acc1), "+{v[32:47]}"(acc2))
        const int tile = rb * 2 + tt, h0 = 4 * hg + 2 * hp;
        auto storeK = [&](int br, const f32x16& a) { const unsigned ko = WS_KV + br * KV_BR + h0 * KV_HEAD + tile * 2048 + lane * 16; st_sc1(rs, ko, pack8(a, 0, 1.f)); st_sc1(rs, ko + KV_HEAD, pack8(a, 8, 1.f)); };
        auto storeV = [&](int br, const f32x16& a) { const unsigned vo = WS_KV + br * KV_BR + (h0 + (l31 >> 4)) * KV_HEAD + tile * 2048 + 1024 + hi * 256 + (l31 & 15) * 16; st_sc1(rs, vo, pack8(a, 0, 1.f)); st_sc1(rs, vo + 512, pack8(a, 8, 1.f)); };
        auto storeQ = [&](int br, const f32x16& a) { const unsigned qo = WS_Q + br * Q_BR + h0 * Q_HEAD + tile * 1024 + lane * 16; st_sc1(rs, qo, pack8(a, 0, QSCALE)); st_sc1(rs, qo + Q_HEAD, pack8(a, 8, QSCALE)); };
        acc0 = f32x16{}; acc1 = f32x16{}; acc2 = f32x16{};
#pragma unroll 1
        for (int i = 0; i < 4; ++i) { QKV_STEP(QKV_STEP_AB); QKV_STEP(QKV_STEP_BA); }
        QKV_FENCE();
        storeK(0, acc1); storeV(0, acc2); storeQ(0, acc0);
        acc0 = f32x16{}; acc1 = f32x16{}; acc2 = f32x16{};
        QKV_STEP(QKV_STEP_AB); QKV_STEP(QKV_STEP_BA);
        WAIT_VM(0);
        if (lane == 0) flag_st(ws, WS_FQ + (((h0 >> 1) * 64) + tile) * 8, tag);
        QKV_STEP(QKV_STEP_AB);
        QKV_STEP(QKV_STEP_BA);
        QKV_STEP(QKV_STEP_AB);
        QKV_STEP(QKV_STEP_BA);
        QKV_STEP(QKV_STEP_AB); QKV_STEP(QKV_STEP_BA);
        asm volatile("s_waitcnt lgkmcnt(0)" ::: "memory");
        QKV_FENCE();
        const int qw = qb * 256 + 64 * wv;
        const unsigned qofs = WS_Q + h * Q_HEAD + (qw / 32) * 1024 + lane * 16;
        { const u32x4 okw = *(volatile LDSP(u32x4))(smem + LDS_SPARE);
          if (!(okw[0] & okw[1] & okw[2] & okw[3])) wait_flags(ws, WS_FQ + (((h >> 1) * 64) + qw / 32 + lane) * 8, lane < 2, tag); }
        qa = ld_sc1(rs, qofs); qbf = ld_sc1(rs, qofs + 1024);
        storeK(1, acc1); storeV(1, acc2); storeQ(1, acc0);
        BARRIER();
        {
            __builtin_amdgcn_s_setprio(2);
            const bf16x8 k0 = lds_b128((lds_cptr)smem + lane * 16);
            f32x16 ca = splat16(-rowmax16(MFMA(k0, qa, splat16(0.f)))), cb = splat16(-rowmax16(MFMA(k0, qbf, splat16(0.f))));
            unsigned kaddr = lds0 + lane * 16, vaddr = lds0 + 1024 + hi * 256 + (l31 & 15) * 16;
            unsigned fill = (l31 == 16 || l31 == 20) ? 0x3f803f80u : 0u;
            unsigned foff = WS_FQ + ((16 + (h0 >> 1)) * 64 + tile) * 8;
            oa = f32x16{}; ob = f32x16{};
            int cnt = 16; const u64 vmask = 0x0000FFFF0000FFFFull;
            asm volatile(ATTN_A_ASM
                : "+{v[0:15]}"(oa), "+{v[16:31]}"(ob), [cnt] "+s"(cnt), "+{v136}"(kaddr), "+{v137}"(vaddr)
                : "{v[32:47]}"(ca), "{v[48:63]}"(cb), "{v[112:115]}"(qa), "{v[116:119]}"(qbf), [vmask] "s"(vmask), "{v138}"(fill), "{v142}"(lds0 + wv * 4), "{v143}"(PROG_MAGIC), "{v139}"(foff), "{v[140:141]}"(tag), [fbase] "s"(ws)
                : ATTN_A_CLOB, "scc", "memory");
            kv0 = WS_KV + h * KV_HEAD; tlo_a = 0; tlo_b = 0; thi = 63; tF = -1; tL = -1; bstep = 0;
            o_off = WS_O + h * Q_HEAD + (qw / 32) * 1024 + lane * 16; f_off = WS_FO + ((qw / 64) * 32 + h) * 8;
        }
    }
    if (__builtin_expect(!(block_ok(oa) && block_ok(ob)), 0)) {
#pragma unroll 1
        for (int blk = 0; blk < 2; ++blk) {
            const f32x16 o = exact_block(rs, kv0 + blk * bstep, blk ? qbf : qa, blk ? tlo_b : tlo_a, thi, tF, tL, lane);
            if (blk) ob = o; else oa = o;
        }
    }
    st_sc1(rs, o_off, pack8(oa, 0, __builtin_amdgcn_rcpf(oa[8]))); st_sc1(rs, o_off + 1024, pack8(ob, 0, __builtin_amdgcn_rcpf(ob[8])));
    WAIT_VM(0);
    if (lane == 0) flag_st(ws, f_off, tag);
    __builtin_amdgcn_s_setprio(0);
    if (wv < 4) {
#pragma unroll 1
        for (unsigned it = 0; it < SPIN_CAP; ++it) { const u32x4 pw = *(volatile LDSP(u32x4))smem; if (pw[0] == PROG_MAGIC && pw[1] == PROG_MAGIC && pw[2] == PROG_MAGIC && pw[3] == PROG_MAGIC) break; }
        const char* wo = (const char*)(obr ? woB : woA) + ocb * 128 + wv * 16 * 8192;
        const unsigned voff = (lane >> 3) * 1024 + (((lane & 7) ^ (((lane >> 4) & 1) << 2)) << 4);
#pragma unroll
        for (int i = 0; i < 16; ++i) glds16<0>(wo + i * 8192, voff, lds0 + LDS_WO + (wv * 16 + i) * 1024);
        WAIT_VM(0);
    }
    BARRIER();
    {
        const int br = obr, cb = ocb, tt = wv & 3, kh = wv >> 2;
        const int tile = orb * 4 + tt;
        const unsigned fo = WS_FO + ((br * 32 + orb * 2) * 32 + lane) * 8;
        const u64 pollv = wv == 0 ? flag_ld(ws, fo) : tag;
        const int q = (lane >> 2) & 3;
        unsigned wb[2];
#pragma unroll
        for (int ct = 0; ct < 2; ++ct) wb[ct] = LDS_WO + (kh * 256 + 4 * hi + q) * 128 + ((ct ^ (q >> 1)) * 64) + ((lane >> 4) & 1) * 32 + (lane & 3) * 8;
        bf16x8 wf[16][2];
#pragma unroll
        for (int i = 0; i < 16; ++i)
#pragma unroll
            for (int ct = 0; ct < 2; ++ct) { lds_cptr p = (lds_cptr)smem + wb[ct] + i * 2048; wf[i][ct] = cat8(lds_tr(p), lds_tr(p + 1024)); }
        __builtin_amdgcn_sched_barrier(0);
        if (!__all(pollv == tag)) wait_flags(ws, fo, true, tag);
        BARRIER();
        bf16x8 of[16];
        const unsigned oofs = WS_O + br * Q_BR + kh * 16 * Q_HEAD + tile * 1024 + lane * 16;
#pragma unroll
        for (int i = 0; i < 16; ++i) of[i] = ld_sc1(rs, oofs + i * Q_HEAD);
        __builtin_amdgcn_sched_barrier(0);
        f32x16 acc[2] = {};
#pragma unroll
        for (int i = 0; i < 16; ++i) { acc[0] = MFMA(wf[i][0], of[i], acc[0]); acc[1] = MFMA(wf[i][1], of[i], acc[1]); }
        typedef float f32x4 __attribute__((ext_vector_type(4)));
        LDSP(f32x4) xs = (LDSP(f32x4))(smem + LDS_XCH) + wv * 256; LDSP(f32x4) xr = (LDSP(f32x4))(smem + LDS_XCH) + (wv ^ 4) * 256;
        const f32x16 snd = kh ? acc[0] : acc[1];
#pragma unroll
        for (int g = 0; g < 4; ++g) xs[g * 64 + lane] = (f32x4){snd[4 * g], snd[4 * g + 1], snd[4 * g + 2], snd[4 * g + 3]};
        BARRIER();
        f32x16 o = kh ? acc[1] : acc[0];
#pragma unroll
        for (int g = 0; g < 4; ++g) { const f32x4 r = xr[g * 64 + lane]; o[4 * g] += r[0]; o[4 * g + 1] += r[1]; o[4 * g + 2] += r[2]; o[4 * g + 3] += r[3]; }
        uint16_t* orow = out + (size_t)(tile * 32 + l31) * 1024 + br * 512 + cb * 64 + kh * 32 + 4 * hi;
#pragma unroll
        for (int g = 0; g < 4; g += 2) {
            unsigned a0 = cvtpk(o[4 * g], o[4 * g + 1]), a1 = cvtpk(o[4 * g + 2], o[4 * g + 3]), b0 = cvtpk(o[4 * g + 4], o[4 * g + 5]), b1 = cvtpk(o[4 * g + 6], o[4 * g + 7]);
            auto r0 = __builtin_amdgcn_permlane32_swap(a0, b0, false, false); auto r1 = __builtin_amdgcn_permlane32_swap(a1, b1, false, false);
            *(u32x4*)(orow - 4 * hi + 8 * g + 8 * hi) = (u32x4){r0[0], r1[0], r0[1], r1[1]};
        }
    }
    asm volatile("fused_code_end:");
}

extern "C" void kernel_launch(void* const* d_in, const int* in_sizes, int n_in, void* d_out, int out_size, void* d_ws, size_t ws_size, hipStream_t stream) {
    (void)hipFuncSetAttribute((const void*)fused_kernel, hipFuncAttributeMaxDynamicSharedMemorySize, LDS_BYTES);
    fused_kernel<<<256, 512, LDS_BYTES, stream>>>((const uint16_t*)d_in[0], (const uint16_t*)d_in[1], (const uint16_t*)d_in[2], (const uint16_t*)d_in[3], (const uint16_t*)d_in[4], (const uint16_t*)d_in[5], (char*)d_ws, (uint16_t*)d_out);
}
```
